```python
import torch

def _softmax(x, axis=-1):
    return torch.softmax(x, dim=axis)
def _gelu(x):
    return torch.nn.functional.gelu(x, approximate="tanh")
def _silu(x):
    return torch.nn.functional.silu(x)
def _sigmoid(x):
    return torch.sigmoid(x)
def _layernorm(x, axis=-1, eps=1e-5):
    m = x.mean(dim=axis, keepdim=True)
    v = x.var(dim=axis, keepdim=True, unbiased=False)
    return (x - m) / torch.sqrt(v + eps)
def _scatter_add(tgt, idx, src, axis=0):
    return tgt.clone().index_add_(axis, idx, src)
def _segment_sum(src, seg, n):
    out = torch.zeros((n,) + src.shape[1:], dtype=src.dtype)
    return out.index_add_(0, seg, src)
def _pad(x, pw):
    flat = tuple(v for pair in reversed(pw) for v in pair)
    return torch.nn.functional.pad(x, flat)

kernel_name = 'gather_scatter__molecular_angular_gemnet__graph'

def setup_inputs():

    _w = (lambda a: (lambda t: t.to(torch.bfloat16) if t.is_floating_point() else t)(torch.as_tensor(a)))
    N, deg, De, L = 10000, 16, 4, 3
    E = N * deg
    T = E * deg
    NR = 8
    dist = torch.rand(E).to(float)
    angle = (torch.rand(T) * torch.pi).to(float)
    centers = torch.linspace(0.0, 1.0, NR).to(float)
    ang_freq = torch.arange(1, L + 1).to(float)
    return {
        "m": _w((lambda *s: torch.randn(*s, dtype=torch.bfloat16) * 0.05)(E, De)),
        "idx_in": _w((lambda lo, hi, *s: torch.randint(lo, hi, s))(0, E, T).to(torch.int64)),
        "idx_out": _w((lambda lo, hi, *s: torch.randint(lo, hi, s))(0, E, T).to(torch.int64)),
        "dist": _w(dist),
        "angle": _w(angle),
        "centers": _w(centers),
        "ang_freq": _w(ang_freq),
        "w_rbf": _w((lambda *s: torch.randn(*s, dtype=torch.bfloat16) * 0.2)(NR, De)),
        "w_sbf": _w((lambda *s: torch.randn(*s, dtype=torch.bfloat16) * 0.2)(NR * L, De)),
        "w_down": _w((lambda *s: torch.randn(*s, dtype=torch.bfloat16) * 0.2)(De, De)),
        "w_up": _w((lambda *s: torch.randn(*s, dtype=torch.bfloat16) * 0.2)(De, De)),
    }

def reference(m, idx_in, idx_out, dist, angle, centers, ang_freq, w_rbf, w_sbf, w_down, w_up):
    E, De = m.shape[0], m.shape[1]
    NR = centers.shape[0]
    L = ang_freq.shape[0]
    rbf = torch.exp(-10.0 * (dist[:, None] - centers[None, :]) ** 2)
    cbf = torch.cos(ang_freq[None, :] * angle[:, None])
    rbf_t = rbf[idx_out]
    sbf = torch.reshape(rbf_t[:, :, None] * cbf[:, None, :], (-1, NR * L))
    x = (m[idx_in] @ w_down) * (sbf @ w_sbf)
    agg = _segment_sum(x, idx_out, E)
    return m + (agg @ w_up) * (rbf @ w_rbf)
```

```cpp
#include <hip/hip_runtime.h>
#include <hip/hip_bf16.h>
#include <stdint.h>

typedef unsigned short u16;
__device__ __forceinline__ float b2f(u16 x) { return __uint_as_float(((unsigned)x) << 16); }
__device__ __forceinline__ float rbf16(float x) { return (float)(__bf16)x; }
__device__ __forceinline__ u16 f2b(float x) { __bf16 h = (__bf16)x; return __builtin_bit_cast(u16, h); }

__global__ void k_scatter(const u16* m, const int* idx_in, const int* idx_out, const u16* dist, const u16* angle,
                          const u16* centers, const u16* freq, const u16* w_sbf, const u16* w_down,
                          float* agg, int T) {
    int t = blockIdx.x * blockDim.x + threadIdx.x;
    if (t >= T) return;
    int i = idx_in[t], o = idx_out[t];
    float a = b2f(angle[t]);
    float d = b2f(dist[o]);
    float cb[3];
    for (int l = 0; l < 3; ++l) cb[l] = rbf16(cosf(rbf16(b2f(freq[l]) * a)));
    float s[4] = {0, 0, 0, 0};
    for (int r = 0; r < 8; ++r) {
        float df = rbf16(d - b2f(centers[r]));
        float rb = rbf16(expf(rbf16(-10.f * rbf16(df * df))));
        for (int l = 0; l < 3; ++l) {
            float sb = rbf16(rb * cb[l]);
            for (int k = 0; k < 4; ++k) s[k] += sb * b2f(w_sbf[(r * 3 + l) * 4 + k]);
        }
    }
    float mm[4];
    for (int j = 0; j < 4; ++j) mm[j] = b2f(m[i * 4 + j]);
    for (int k = 0; k < 4; ++k) {
        float md = 0;
        for (int j = 0; j < 4; ++j) md += mm[j] * b2f(w_down[j * 4 + k]);
        float x = rbf16(rbf16(md) * rbf16(s[k]));
        atomicAdd(&agg[o * 4 + k], x);
    }
}

__global__ void k_epi(const u16* m, const u16* dist, const u16* centers, const u16* w_rbf, const u16* w_up,
                      const float* agg, u16* out, int E) {
    int e = blockIdx.x * blockDim.x + threadIdx.x;
    if (e >= E) return;
    float d = b2f(dist[e]);
    float g[4] = {0, 0, 0, 0};
    for (int r = 0; r < 8; ++r) {
        float df = rbf16(d - b2f(centers[r]));
        float rb = rbf16(expf(rbf16(-10.f * rbf16(df * df))));
        for (int k = 0; k < 4; ++k) g[k] += rb * b2f(w_rbf[r * 4 + k]);
    }
    float a[4];
    for (int j = 0; j < 4; ++j) a[j] = rbf16(agg[e * 4 + j]);
    for (int k = 0; k < 4; ++k) {
        float u = 0;
        for (int j = 0; j < 4; ++j) u += a[j] * b2f(w_up[j * 4 + k]);
        out[e * 4 + k] = f2b(b2f(m[e * 4 + k]) + rbf16(rbf16(u) * rbf16(g[k])));
    }
}

extern "C" void kernel_launch(void* const* d_in, const int* in_sizes, int n_in,
                              void* d_out, int out_size, void* d_ws, size_t ws_size,
                              hipStream_t stream) {
    const u16* m = (const u16*)d_in[0];
    const int* idx_in = (const int*)d_in[1];
    const int* idx_out = (const int*)d_in[2];
    const u16* dist = (const u16*)d_in[3];
    const u16* angle = (const u16*)d_in[4];
    const u16* centers = (const u16*)d_in[5];
    const u16* freq = (const u16*)d_in[6];
    const u16* w_rbf = (const u16*)d_in[7];
    const u16* w_sbf = (const u16*)d_in[8];
    const u16* w_down = (const u16*)d_in[9];
    const u16* w_up = (const u16*)d_in[10];
    int E = in_sizes[3], T = in_sizes[1];
    float* agg = (float*)d_ws;
    hipMemsetAsync(agg, 0, (size_t)E * 16, stream);
    k_scatter<<<(T + 255) / 256, 256, 0, stream>>>(m, idx_in, idx_out, dist, angle, centers, freq, w_sbf, w_down, agg, T);
    k_epi<<<(E + 255) / 256, 256, 0, stream>>>(m, dist, centers, w_rbf, w_up, agg, (u16*)d_out, E);
}
```

```cpp
#include <hip/hip_runtime.h>
#include <hip/hip_bf16.h>
#include <stdint.h>

typedef unsigned short u16;
typedef unsigned int u32;
typedef u32 u32x2 __attribute__((ext_vector_type(2)));
typedef u32 u32x3 __attribute__((ext_vector_type(3)));
typedef u32 u32x4 __attribute__((ext_vector_type(4)));
typedef float f32x2 __attribute__((ext_vector_type(2)));
typedef float f32x4 __attribute__((ext_vector_type(4)));
typedef __bf16 bf16x2 __attribute__((ext_vector_type(2)));

__device__ __forceinline__ float b2f(u16 x) { return __uint_as_float(((u32)x) << 16); }
__device__ __forceinline__ float lo2f(u32 x) { return __uint_as_float(x << 16); }
__device__ __forceinline__ float hi2f(u32 x) { return __uint_as_float(x & 0xffff0000u); }
__device__ __forceinline__ float wel(const u32* __restrict__ w, int i) { return (i & 1) ? hi2f(w[i >> 1]) : lo2f(w[i >> 1]); }
__device__ __forceinline__ float rbf16(float x) { return (float)(__bf16)x; }
__device__ __forceinline__ u16 f2b(float x) { __bf16 h = (__bf16)x; return __builtin_bit_cast(u16, h); }
__device__ __forceinline__ u32 pk2(float a, float b) {
    bf16x2 h; h.x = (__bf16)a; h.y = (__bf16)b; return __builtin_bit_cast(u32, h);
}
__device__ __forceinline__ float fcos(float x) {
    return __builtin_amdgcn_cosf(__builtin_amdgcn_fractf(x * 0.15915494309189535f));
}

#define NE 160000
#define NT 2560000
#define NB 256
#define EPB 625
#define TPB 10000
#define REGION 61568
#define WS_MD 0
#define WS_DESC (1280000)
#define WS_REC (1280000 + 262144)
#define XCAP 10752
#define NSLOT 9

__device__ __forceinline__ u32 wave_incl_scan(u32 v) {
    v += __builtin_amdgcn_update_dpp(0u, v, 0x111, 0xf, 0xf, false);
    v += __builtin_amdgcn_update_dpp(0u, v, 0x112, 0xf, 0xf, false);
    v += __builtin_amdgcn_update_dpp(0u, v, 0x114, 0xf, 0xf, false);
    v += __builtin_amdgcn_update_dpp(0u, v, 0x118, 0xf, 0xf, false);
    v += __builtin_amdgcn_update_dpp(0u, v, 0x142, 0xa, 0xf, false);
    v += __builtin_amdgcn_update_dpp(0u, v, 0x143, 0xc, 0xf, false);
    return v;
}

__global__ __launch_bounds__(1024) void k_part(const u32x2* __restrict__ m, const int* __restrict__ idx_in,
                                               const int* __restrict__ idx_out, const u16* __restrict__ angle,
                                               const u32* __restrict__ w_down, char* __restrict__ ws) {
    __shared__ __attribute__((aligned(16))) u32 s_cnt[NB];
    __shared__ __attribute__((aligned(16))) u32 s_base[NB + 4];
    __shared__ __attribute__((aligned(16))) char s_rec[REGION];
    const int tid = threadIdx.x, b = blockIdx.x;
    const int t0 = b * TPB;
    const bool hasC = tid < 904;
    const int tc = t0 + 8192 + (hasC ? tid * 2 : 0);
    u32x2 mrow = m[b * EPB + (tid < EPB ? tid : 0)];
    u32x4 oA = *(const u32x4*)(idx_out + t0 + tid * 4);
    u32x4 oB = *(const u32x4*)(idx_out + t0 + 4096 + tid * 4);
    u32x2 oC = *(const u32x2*)(idx_out + tc);
    u32x4 iA = *(const u32x4*)(idx_in + t0 + tid * 4);
    u32x4 iB = *(const u32x4*)(idx_in + t0 + 4096 + tid * 4);
    u32x2 iC = *(const u32x2*)(idx_in + tc);
    u32x2 aA = *(const u32x2*)(angle + t0 + tid * 4);
    u32x2 aB = *(const u32x2*)(angle + t0 + 4096 + tid * 4);
    u32 aC = *(const u32*)(angle + tc);
    if (tid < NB) s_cnt[tid] = 0;
    __syncthreads();
    u32 o[10] = {oA.x, oA.y, oA.z, oA.w, oB.x, oB.y, oB.z, oB.w, oC.x, oC.y};
    u32 bk[10], rk[10];
#pragma unroll
    for (int k = 0; k < 10; ++k) {
        bk[k] = __umulhi(o[k], 6871948u);
        o[k] -= bk[k] * EPB;
        if (k < 8 || hasC) rk[k] = atomicAdd(&s_cnt[bk[k]], 1u);
    }
    __syncthreads();
    if (tid < 64) {
        u32x4 c = *(const u32x4*)&s_cnt[tid * 4];
        u32 p0 = (c.x + 1) >> 1, p1 = (c.y + 1) >> 1, p2 = (c.z + 1) >> 1, p3 = (c.w + 1) >> 1;
        u32 tot = p0 + p1 + p2 + p3;
        u32 inc = wave_incl_scan(tot);
        u32 e0 = inc - tot, e1 = e0 + p0, e2 = e1 + p1, e3 = e2 + p2;
        *(u32x4*)&s_base[tid * 4] = (u32x4){e0 * 12, e1 * 12, e2 * 12, e3 * 12};
        if (tid == 63) s_base[NB] = inc * 12;
        *(u32x4*)(ws + WS_DESC + b * (NB * 4) + tid * 16) =
            (u32x4){e0 | (c.x << 16), e1 | (c.y << 16), e2 | (c.z << 16), e3 | (c.w << 16)};
    }
    if (tid < EPB) {
        float mm[4] = {lo2f(mrow.x), hi2f(mrow.x), lo2f(mrow.y), hi2f(mrow.y)};
        float d[4];
#pragma unroll
        for (int k = 0; k < 4; ++k) {
            float s = 0.f;
#pragma unroll
            for (int j = 0; j < 4; ++j) s = fmaf(mm[j], wel(w_down, j * 4 + k), s);
            d[k] = s;
        }
        *(u32x2*)(ws + WS_MD + (size_t)(b * EPB + tid) * 8) = (u32x2){pk2(d[0], d[1]), pk2(d[2], d[3])};
    }
    __syncthreads();
    u32 in[10] = {iA.x, iA.y, iA.z, iA.w, iB.x, iB.y, iB.z, iB.w, iC.x, iC.y};
    u32 an[5] = {aA.x, aA.y, aB.x, aB.y, aC};
    u32 ad[10];
#pragma unroll
    for (int k = 0; k < 10; ++k) {
        if (k < 8 || hasC) {
            u32 base = s_base[bk[k]];
            ad[k] = base + (rk[k] >> 1) * 12;
            *(u32*)(s_rec + ad[k] + (rk[k] & 1) * 4) = in[k] | (o[k] << 18);
        }
    }
#pragma unroll
    for (int k = 0; k < 10; ++k) {
        if (k < 8 || hasC) {
            u16 a = (k & 1) ? (u16)(an[k >> 1] >> 16) : (u16)an[k >> 1];
            *(u16*)(s_rec + ad[k] + 8 + (rk[k] & 1) * 2) = a;
        }
    }
    __syncthreads();
    const u32 nbytes = s_base[NB];
    char* dst = ws + WS_REC + (size_t)b * REGION;
#pragma unroll
    for (int i = 0; i < 4; ++i) {
        u32 off = (i * 1024 + tid) * 16;
        if (off < nbytes) *(u32x4*)(dst + off) = *(const u32x4*)(s_rec + off);
    }
}

__device__ __forceinline__ u32x2 make_x(u32x2 md, u32 ang_hi  , const float* sG, u32 lo,
                                        float f0, float f1, float f2) {
    float a = __uint_as_float(ang_hi);
    u32 t01 = pk2(f0 * a, f1 * a);
    float x2 = rbf16(f2 * a);
    u32 c01 = pk2(fcos(lo2f(t01)), fcos(hi2f(t01)));
    float c2 = rbf16(fcos(x2));
    float c0 = lo2f(c01), c1 = hi2f(c01);
    const f32x4* g = (const f32x4*)(sG + lo * 12);
    f32x4 g0 = g[0], g1 = g[1], g2 = g[2];
    f32x4 s = g0 * c0 + g1 * c1 + g2 * c2;
    u32 s01 = pk2(s.x, s.y), s23 = pk2(s.z, s.w);
    return (u32x2){pk2(lo2f(md.x) * lo2f(s01), hi2f(md.x) * hi2f(s01)), pk2(lo2f(md.y) * lo2f(s23), hi2f(md.y) * hi2f(s23))};
}
__device__ __forceinline__ void ovf_add(double* sO, u32 lo, u32x2 x) {
    atomicAdd(&sO[lo], (double)lo2f(x.x));
    atomicAdd(&sO[lo + 640], (double)hi2f(x.x));
    atomicAdd(&sO[lo + 1280], (double)lo2f(x.y));
    atomicAdd(&sO[lo + 1920], (double)hi2f(x.y));
}

__global__ __launch_bounds__(1024) void k_acc(const u32x2* __restrict__ m, const u16* __restrict__ dist,
                                              const u32* __restrict__ centers, const u16* __restrict__ freq,
                                              const u32* __restrict__ w_rbf, const u32* __restrict__ w_sbf,
                                              const u32* __restrict__ w_up, const char* __restrict__ ws,
                                              u32x2* __restrict__ out) {
    __shared__ __attribute__((aligned(16))) u32 s_cnt[640];
    __shared__ __attribute__((aligned(16))) u32 s_base[640];
    __shared__ __attribute__((aligned(16))) float s_G[EPB * 12];
    __shared__ __attribute__((aligned(16))) double s_ovf[640 * 4];
    __shared__ __attribute__((aligned(16))) u32x2 s_x[XCAP];
    const int tid = threadIdx.x;
    const int j = (blockIdx.x & 7) * 32 + (blockIdx.x >> 3);
    const int src = tid >> 2, sub = tid & 3;
    const u32 f01 = *(const u32*)freq;
    const float f0 = lo2f(f01), f1 = hi2f(f01), f2 = lo2f(((const u32*)freq)[1]);
    const u32 desc = *(const u32*)(ws + WS_DESC + src * (NB * 4) + j * 4);
    const int e = j * EPB + (tid < EPB ? tid : 0);
    const u16 dist_e = dist[e];
    const u32x2 mrow = m[e];
    if (tid < 640) {
        s_cnt[tid] = 0;
        s_ovf[tid] = 0.0; s_ovf[tid + 640] = 0.0; s_ovf[tid + 1280] = 0.0; s_ovf[tid + 1920] = 0.0;
    }
    const u32 len = desc >> 16;
    const u32 npair = (len + 1) >> 1;
    const char* rbase = ws + WS_REC + (size_t)src * REGION + (desc & 0xffffu) * 12;
    const u32x2* md = (const u32x2*)(ws + WS_MD);
    u32x3 rec[NSLOT];
#pragma unroll
    for (int k = 0; k < NSLOT; ++k) {
        u32 p = sub + 4 * k;
        rec[k] = *(const u32x3*)(rbase + (p < npair ? p : 0u) * 12);
    }
    float gate[4];
    if (tid < EPB) {
        float d = b2f(dist_e);
        float G[12], g[4];
#pragma unroll
        for (int i = 0; i < 12; ++i) G[i] = 0.f;
#pragma unroll
        for (int i = 0; i < 4; ++i) g[i] = 0.f;
#pragma unroll
        for (int r = 0; r < 8; ++r) {
            float df = rbf16(d - wel(centers, r));
            float rb = rbf16(__expf(rbf16(-10.f * rbf16(df * df))));
#pragma unroll
            for (int i = 0; i < 12; ++i) G[i] = fmaf(rb, wel(w_sbf, r * 12 + i), G[i]);
#pragma unroll
            for (int i = 0; i < 4; ++i) g[i] = fmaf(rb, wel(w_rbf, r * 4 + i), g[i]);
        }
#pragma unroll
        for (int i = 0; i < 3; ++i) *(f32x4*)&s_G[tid * 12 + i * 4] = (f32x4){G[i * 4], G[i * 4 + 1], G[i * 4 + 2], G[i * 4 + 3]};
#pragma unroll
        for (int i = 0; i < 4; ++i) gate[i] = rbf16(g[i]);
    }
        u32x2 mv[NSLOT][2];
#pragma unroll
    for (int k = 0; k < NSLOT; ++k) {
        u32 p = sub + 4 * k;
        mv[k][0] = md[(2 * p < len) ? (rec[k].x & 0x3ffffu) : 0u];
        mv[k][1] = md[(2 * p + 1 < len) ? (rec[k].y & 0x3ffffu) : 0u];
    }
    __syncthreads();
#pragma unroll
    for (int k = 0; k < NSLOT; ++k) {
        u32 p = sub + 4 * k;
        u32 l0 = rec[k].x >> 18, l1 = rec[k].y >> 18;
        if (2 * p < len) rec[k].x = l0 | (atomicAdd(&s_cnt[l0], 1u) << 10);
        if (2 * p + 1 < len) rec[k].y = l1 | (atomicAdd(&s_cnt[l1], 1u) << 10);
    }
    __syncthreads();
    if (tid < 64) {
        u32 c[10];
#pragma unroll
        for (int i = 0; i < 5; ++i) { u32x2 v = *(const u32x2*)&s_cnt[tid * 10 + i * 2]; c[i * 2] = v.x; c[i * 2 + 1] = v.y; }
        u32 tot = 0;
#pragma unroll
        for (int i = 0; i < 10; ++i) { u32 t = c[i]; c[i] = tot; tot += t; }
        u32 ex = wave_incl_scan(tot) - tot;
#pragma unroll
        for (int i = 0; i < 5; ++i) *(u32x2*)&s_base[tid * 10 + i * 2] = (u32x2){c[i * 2] + ex, c[i * 2 + 1] + ex};
    }
    __syncthreads();
#pragma unroll
    for (int k = 0; k < NSLOT; ++k) {
        u32 p = sub + 4 * k;
        if (2 * p < len) {
            u32 lo = rec[k].x & 1023u;
            u32 pos = s_base[lo] + (rec[k].x >> 10);
            u32x2 x = make_x(mv[k][0], rec[k].z << 16, s_G, lo, f0, f1, f2);
            if (pos < XCAP) s_x[pos] = x; else ovf_add(s_ovf, lo, x);
        }
        if (2 * p + 1 < len) {
            u32 lo = rec[k].y & 1023u;
            u32 pos = s_base[lo] + (rec[k].y >> 10);
            u32x2 x = make_x(mv[k][1], rec[k].z & 0xffff0000u, s_G, lo, f0, f1, f2);
            if (pos < XCAP) s_x[pos] = x; else ovf_add(s_ovf, lo, x);
        }
    }
    for (u32 p = sub + 4 * NSLOT; p < npair; p += 4) {
        u32x3 r = *(const u32x3*)(rbase + p * 12);
        ovf_add(s_ovf, r.x >> 18, make_x(md[r.x & 0x3ffffu], r.z << 16, s_G, r.x >> 18, f0, f1, f2));
        if (2 * p + 1 < len) ovf_add(s_ovf, r.y >> 18, make_x(md[r.y & 0x3ffffu], r.z & 0xffff0000u, s_G, r.y >> 18, f0, f1, f2));
    }
    __syncthreads();
    if (tid < EPB) {
        u32 q = s_base[tid];
        u32 qe = min(q + s_cnt[tid], (u32)XCAP);
        float a0 = (float)s_ovf[tid], a1 = (float)s_ovf[tid + 640], a2 = (float)s_ovf[tid + 1280], a3 = (float)s_ovf[tid + 1920];
        for (; q + 4 <= qe; q += 4) {
            u32x2 v0 = s_x[q], v1 = s_x[q + 1], v2 = s_x[q + 2], v3 = s_x[q + 3];
            a0 += (lo2f(v0.x) + lo2f(v1.x)) + (lo2f(v2.x) + lo2f(v3.x));
            a1 += (hi2f(v0.x) + hi2f(v1.x)) + (hi2f(v2.x) + hi2f(v3.x));
            a2 += (lo2f(v0.y) + lo2f(v1.y)) + (lo2f(v2.y) + lo2f(v3.y));
            a3 += (hi2f(v0.y) + hi2f(v1.y)) + (hi2f(v2.y) + hi2f(v3.y));
        }
        for (; q < qe; ++q) {
            u32x2 v = s_x[q];
            a0 += lo2f(v.x); a1 += hi2f(v.x); a2 += lo2f(v.y); a3 += hi2f(v.y);
        }
        float ag[4] = {rbf16(a0), rbf16(a1), rbf16(a2), rbf16(a3)};
        float mm[4] = {lo2f(mrow.x), hi2f(mrow.x), lo2f(mrow.y), hi2f(mrow.y)};
        float res[4];
#pragma unroll
        for (int k = 0; k < 4; ++k) {
            float u = 0.f;
#pragma unroll
            for (int i = 0; i < 4; ++i) u = fmaf(ag[i], wel(w_up, i * 4 + k), u);
            res[k] = mm[k] + rbf16(rbf16(u) * gate[k]);
        }
        out[e] = (u32x2){pk2(res[0], res[1]), pk2(res[2], res[3])};
    }
}

extern "C" void kernel_launch(void* const* d_in, const int* in_sizes, int n_in,
                              void* d_out, int out_size, void* d_ws, size_t ws_size,
                              hipStream_t stream) {
    const u16* m = (const u16*)d_in[0];
    const int* idx_in = (const int*)d_in[1];
    const int* idx_out = (const int*)d_in[2];
    const u16* dist = (const u16*)d_in[3];
    const u16* angle = (const u16*)d_in[4];
    const u16* centers = (const u16*)d_in[5];
    const u16* freq = (const u16*)d_in[6];
    const u16* w_rbf = (const u16*)d_in[7];
    const u16* w_sbf = (const u16*)d_in[8];
    const u16* w_down = (const u16*)d_in[9];
    const u16* w_up = (const u16*)d_in[10];
    k_part<<<NB, 1024, 0, stream>>>((const u32x2*)m, idx_in, idx_out, angle, (const u32*)w_down, (char*)d_ws);
    k_acc<<<NB, 1024, 0, stream>>>((const u32x2*)m, dist, (const u32*)centers, freq, (const u32*)w_rbf, (const u32*)w_sbf, (const u32*)w_up, (const char*)d_ws, (u32x2*)d_out);
}
```

```cpp
#include <hip/hip_runtime.h>
#include <hip/hip_bf16.h>
#include <stdint.h>

typedef unsigned short u16;
typedef unsigned int u32;
typedef u32 u32x2 __attribute__((ext_vector_type(2)));
typedef u32 u32x3 __attribute__((ext_vector_type(3)));
typedef u32 u32x4 __attribute__((ext_vector_type(4)));
typedef float f32x2 __attribute__((ext_vector_type(2)));
typedef float f32x4 __attribute__((ext_vector_type(4)));
typedef __bf16 bf16x2 __attribute__((ext_vector_type(2)));

__device__ __forceinline__ float b2f(u16 x) { return __uint_as_float(((u32)x) << 16); }
__device__ __forceinline__ float lo2f(u32 x) { return __uint_as_float(x << 16); }
__device__ __forceinline__ float hi2f(u32 x) { return __uint_as_float(x & 0xffff0000u); }
__device__ __forceinline__ float wel(const u32* __restrict__ w, int i) { return (i & 1) ? hi2f(w[i >> 1]) : lo2f(w[i >> 1]); }
__device__ __forceinline__ float rbf16(float x) { return (float)(__bf16)x; }
__device__ __forceinline__ u16 f2b(float x) { __bf16 h = (__bf16)x; return __builtin_bit_cast(u16, h); }
__device__ __forceinline__ u32 pk2(float a, float b) {
    bf16x2 h; h.x = (__bf16)a; h.y = (__bf16)b; return __builtin_bit_cast(u32, h);
}
__device__ __forceinline__ float fcos(float x) {
    return __builtin_amdgcn_cosf(__builtin_amdgcn_fractf(x * 0.15915494309189535f));
}

#define PROF
#define NSTAMP 10
#define STAMP(K, i) do { if (threadIdx.x == 0) ((unsigned long long*)(ws + ((size_t)200 << 20)))[((K) * 256 + blockIdx.x) * 16 + (i)] = __builtin_amdgcn_s_memrealtime(); } while (0)
#define NE 160000
#define NT 2560000
#define NB 256
#define EPB 625
#define TPB 10000
#define REGION 61568
#define WS_MD 0
#define WS_DESC (1280000)
#define WS_REC (1280000 + 262144)
#define XCAP 10752
#define NSLOT 9

__device__ __forceinline__ u32 wave_incl_scan(u32 v) {
    v += __builtin_amdgcn_update_dpp(0u, v, 0x111, 0xf, 0xf, false);
    v += __builtin_amdgcn_update_dpp(0u, v, 0x112, 0xf, 0xf, false);
    v += __builtin_amdgcn_update_dpp(0u, v, 0x114, 0xf, 0xf, false);
    v += __builtin_amdgcn_update_dpp(0u, v, 0x118, 0xf, 0xf, false);
    v += __builtin_amdgcn_update_dpp(0u, v, 0x142, 0xa, 0xf, false);
    v += __builtin_amdgcn_update_dpp(0u, v, 0x143, 0xc, 0xf, false);
    return v;
}

__global__ __launch_bounds__(1024) void k_part(const u32x2* __restrict__ m, const int* __restrict__ idx_in,
                                               const int* __restrict__ idx_out, const u16* __restrict__ angle,
                                               const u32* __restrict__ w_down, char* __restrict__ ws) {
    __shared__ __attribute__((aligned(16))) u32 s_cnt[NB];
    __shared__ __attribute__((aligned(16))) u32 s_base[NB + 4];
    __shared__ __attribute__((aligned(16))) char s_rec[REGION];
    const int tid = threadIdx.x, b = blockIdx.x;
    STAMP(0, 0);
    const int t0 = b * TPB;
    const bool hasC = tid < 904;
    const int tc = t0 + 8192 + (hasC ? tid * 2 : 0);
    u32x2 mrow = m[b * EPB + (tid < EPB ? tid : 0)];
    u32x4 oA = *(const u32x4*)(idx_out + t0 + tid * 4);
    u32x4 oB = *(const u32x4*)(idx_out + t0 + 4096 + tid * 4);
    u32x2 oC = *(const u32x2*)(idx_out + tc);
    u32x4 iA = *(const u32x4*)(idx_in + t0 + tid * 4);
    u32x4 iB = *(const u32x4*)(idx_in + t0 + 4096 + tid * 4);
    u32x2 iC = *(const u32x2*)(idx_in + tc);
    u32x2 aA = *(const u32x2*)(angle + t0 + tid * 4);
    u32x2 aB = *(const u32x2*)(angle + t0 + 4096 + tid * 4);
    u32 aC = *(const u32*)(angle + tc);
    if (tid < NB) s_cnt[tid] = 0;
    __syncthreads();
    STAMP(0, 1);
    u32 o[10] = {oA.x, oA.y, oA.z, oA.w, oB.x, oB.y, oB.z, oB.w, oC.x, oC.y};
    u32 bk[10], rk[10];
#pragma unroll
    for (int k = 0; k < 10; ++k) {
        bk[k] = __umulhi(o[k], 6871948u);
        o[k] -= bk[k] * EPB;
        if (k < 8 || hasC) rk[k] = atomicAdd(&s_cnt[bk[k]], 1u);
    }
    __syncthreads();
    STAMP(0, 2);
    if (tid < 64) {
        u32x4 c = *(const u32x4*)&s_cnt[tid * 4];
        u32 p0 = (c.x + 1) >> 1, p1 = (c.y + 1) >> 1, p2 = (c.z + 1) >> 1, p3 = (c.w + 1) >> 1;
        u32 tot = p0 + p1 + p2 + p3;
        u32 inc = wave_incl_scan(tot);
        u32 e0 = inc - tot, e1 = e0 + p0, e2 = e1 + p1, e3 = e2 + p2;
        *(u32x4*)&s_base[tid * 4] = (u32x4){e0 * 12, e1 * 12, e2 * 12, e3 * 12};
        if (tid == 63) s_base[NB] = inc * 12;
        *(u32x4*)(ws + WS_DESC + b * (NB * 4) + tid * 16) =
            (u32x4){e0 | (c.x << 16), e1 | (c.y << 16), e2 | (c.z << 16), e3 | (c.w << 16)};
    }
    if (tid < EPB) {
        float mm[4] = {lo2f(mrow.x), hi2f(mrow.x), lo2f(mrow.y), hi2f(mrow.y)};
        float d[4];
#pragma unroll
        for (int k = 0; k < 4; ++k) {
            float s = 0.f;
#pragma unroll
            for (int j = 0; j < 4; ++j) s = fmaf(mm[j], wel(w_down, j * 4 + k), s);
            d[k] = s;
        }
        *(u32x2*)(ws + WS_MD + (size_t)(b * EPB + tid) * 8) = (u32x2){pk2(d[0], d[1]), pk2(d[2], d[3])};
    }
    __syncthreads();
    STAMP(0, 3);
    u32 in[10] = {iA.x, iA.y, iA.z, iA.w, iB.x, iB.y, iB.z, iB.w, iC.x, iC.y};
    u32 an[5] = {aA.x, aA.y, aB.x, aB.y, aC};
    u32 ad[10];
#pragma unroll
    for (int k = 0; k < 10; ++k) {
        if (k < 8 || hasC) {
            u32 base = s_base[bk[k]];
            ad[k] = base + (rk[k] >> 1) * 12;
            *(u32*)(s_rec + ad[k] + (rk[k] & 1) * 4) = in[k] | (o[k] << 18);
        }
    }
#pragma unroll
    for (int k = 0; k < 10; ++k) {
        if (k < 8 || hasC) {
            u16 a = (k & 1) ? (u16)(an[k >> 1] >> 16) : (u16)an[k >> 1];
            *(u16*)(s_rec + ad[k] + 8 + (rk[k] & 1) * 2) = a;
        }
    }
    __syncthreads();
    STAMP(0, 4);
    const u32 nbytes = s_base[NB];
    char* dst = ws + WS_REC + (size_t)b * REGION;
#pragma unroll
    for (int i = 0; i < 4; ++i) {
        u32 off = (i * 1024 + tid) * 16;
        if (off < nbytes) *(u32x4*)(dst + off) = *(const u32x4*)(s_rec + off);
    }
    STAMP(0, 5);
    asm volatile("s_waitcnt vmcnt(0)");
    __syncthreads();
    STAMP(0, 6);
}

__device__ __forceinline__ u32x2 make_x(u32x2 md, u32 ang_hi  , const float* sG, u32 lo,
                                        float f0, float f1, float f2) {
    float a = __uint_as_float(ang_hi);
    u32 t01 = pk2(f0 * a, f1 * a);
    float x2 = rbf16(f2 * a);
    u32 c01 = pk2(fcos(lo2f(t01)), fcos(hi2f(t01)));
    float c2 = rbf16(fcos(x2));
    float c0 = lo2f(c01), c1 = hi2f(c01);
    const f32x4* g = (const f32x4*)(sG + lo * 12);
    f32x4 g0 = g[0], g1 = g[1], g2 = g[2];
    f32x4 s = g0 * c0 + g1 * c1 + g2 * c2;
    u32 s01 = pk2(s.x, s.y), s23 = pk2(s.z, s.w);
    return (u32x2){pk2(lo2f(md.x) * lo2f(s01), hi2f(md.x) * hi2f(s01)), pk2(lo2f(md.y) * lo2f(s23), hi2f(md.y) * hi2f(s23))};
}
__device__ __forceinline__ void ovf_add(double* sO, u32 lo, u32x2 x) {
    atomicAdd(&sO[lo], (double)lo2f(x.x));
    atomicAdd(&sO[lo + 640], (double)hi2f(x.x));
    atomicAdd(&sO[lo + 1280], (double)lo2f(x.y));
    atomicAdd(&sO[lo + 1920], (double)hi2f(x.y));
}

__global__ __launch_bounds__(1024) void k_acc(const u32x2* __restrict__ m, const u16* __restrict__ dist,
                                              const u32* __restrict__ centers, const u16* __restrict__ freq,
                                              const u32* __restrict__ w_rbf, const u32* __restrict__ w_sbf,
                                              const u32* __restrict__ w_up, char* __restrict__ ws,
                                              u32x2* __restrict__ out) {
    __shared__ __attribute__((aligned(16))) u32 s_cnt[640];
    __shared__ __attribute__((aligned(16))) u32 s_base[640];
    __shared__ __attribute__((aligned(16))) float s_G[EPB * 12];
    __shared__ __attribute__((aligned(16))) double s_ovf[640 * 4];
    __shared__ __attribute__((aligned(16))) u32x2 s_x[XCAP];
    const int tid = threadIdx.x;
    const int j = (blockIdx.x & 7) * 32 + (blockIdx.x >> 3);
    STAMP(1, 0);
    const int src = tid >> 2, sub = tid & 3;
    const u32 f01 = *(const u32*)freq;
    const float f0 = lo2f(f01), f1 = hi2f(f01), f2 = lo2f(((const u32*)freq)[1]);
    const u32 desc = *(const u32*)(ws + WS_DESC + src * (NB * 4) + j * 4);
    const int e = j * EPB + (tid < EPB ? tid : 0);
    const u16 dist_e = dist[e];
    const u32x2 mrow = m[e];
    if (tid < 640) {
        s_cnt[tid] = 0;
        s_ovf[tid] = 0.0; s_ovf[tid + 640] = 0.0; s_ovf[tid + 1280] = 0.0; s_ovf[tid + 1920] = 0.0;
    }
    const u32 len = desc >> 16;
    const u32 npair = (len + 1) >> 1;
    const char* rbase = ws + WS_REC + (size_t)src * REGION + (desc & 0xffffu) * 12;
    const u32x2* md = (const u32x2*)(ws + WS_MD);
    u32x3 rec[NSLOT];
#pragma unroll
    for (int k = 0; k < NSLOT; ++k) {
        u32 p = sub + 4 * k;
        rec[k] = *(const u32x3*)(rbase + (p < npair ? p : 0u) * 12);
    }
    STAMP(1, 1);
    float gate[4];
    if (tid < EPB) {
        float d = b2f(dist_e);
        float G[12], g[4];
#pragma unroll
        for (int i = 0; i < 12; ++i) G[i] = 0.f;
#pragma unroll
        for (int i = 0; i < 4; ++i) g[i] = 0.f;
#pragma unroll
        for (int r = 0; r < 8; ++r) {
            float df = rbf16(d - wel(centers, r));
            float rb = rbf16(__expf(rbf16(-10.f * rbf16(df * df))));
#pragma unroll
            for (int i = 0; i < 12; ++i) G[i] = fmaf(rb, wel(w_sbf, r * 12 + i), G[i]);
#pragma unroll
            for (int i = 0; i < 4; ++i) g[i] = fmaf(rb, wel(w_rbf, r * 4 + i), g[i]);
        }
#pragma unroll
        for (int i = 0; i < 3; ++i) *(f32x4*)&s_G[tid * 12 + i * 4] = (f32x4){G[i * 4], G[i * 4 + 1], G[i * 4 + 2], G[i * 4 + 3]};
#pragma unroll
        for (int i = 0; i < 4; ++i) gate[i] = rbf16(g[i]);
    }
        STAMP(1, 2);
    u32x2 mv[NSLOT][2];
#pragma unroll
    for (int k = 0; k < NSLOT; ++k) {
        u32 p = sub + 4 * k;
        mv[k][0] = md[(2 * p < len) ? (rec[k].x & 0x3ffffu) : 0u];
        mv[k][1] = md[(2 * p + 1 < len) ? (rec[k].y & 0x3ffffu) : 0u];
    }
    STAMP(1, 3);
    __syncthreads();
    STAMP(1, 4);
#pragma unroll
    for (int k = 0; k < NSLOT; ++k) {
        u32 p = sub + 4 * k;
        u32 l0 = rec[k].x >> 18, l1 = rec[k].y >> 18;
        if (2 * p < len) rec[k].x = l0 | (atomicAdd(&s_cnt[l0], 1u) << 10);
        if (2 * p + 1 < len) rec[k].y = l1 | (atomicAdd(&s_cnt[l1], 1u) << 10);
    }
    __syncthreads();
    STAMP(1, 5);
    if (tid < 64) {
        u32 c[10];
#pragma unroll
        for (int i = 0; i < 5; ++i) { u32x2 v = *(const u32x2*)&s_cnt[tid * 10 + i * 2]; c[i * 2] = v.x; c[i * 2 + 1] = v.y; }
        u32 tot = 0;
#pragma unroll
        for (int i = 0; i < 10; ++i) { u32 t = c[i]; c[i] = tot; tot += t; }
        u32 ex = wave_incl_scan(tot) - tot;
#pragma unroll
        for (int i = 0; i < 5; ++i) *(u32x2*)&s_base[tid * 10 + i * 2] = (u32x2){c[i * 2] + ex, c[i * 2 + 1] + ex};
    }
    __syncthreads();
    STAMP(1, 6);
#pragma unroll
    for (int k = 0; k < NSLOT; ++k) {
        u32 p = sub + 4 * k;
        if (2 * p < len) {
            u32 lo = rec[k].x & 1023u;
            u32 pos = s_base[lo] + (rec[k].x >> 10);
            u32x2 x = make_x(mv[k][0], rec[k].z << 16, s_G, lo, f0, f1, f2);
            if (pos < XCAP) s_x[pos] = x; else ovf_add(s_ovf, lo, x);
        }
        if (2 * p + 1 < len) {
            u32 lo = rec[k].y & 1023u;
            u32 pos = s_base[lo] + (rec[k].y >> 10);
            u32x2 x = make_x(mv[k][1], rec[k].z & 0xffff0000u, s_G, lo, f0, f1, f2);
            if (pos < XCAP) s_x[pos] = x; else ovf_add(s_ovf, lo, x);
        }
    }
    for (u32 p = sub + 4 * NSLOT; p < npair; p += 4) {
        u32x3 r = *(const u32x3*)(rbase + p * 12);
        ovf_add(s_ovf, r.x >> 18, make_x(md[r.x & 0x3ffffu], r.z << 16, s_G, r.x >> 18, f0, f1, f2));
        if (2 * p + 1 < len) ovf_add(s_ovf, r.y >> 18, make_x(md[r.y & 0x3ffffu], r.z & 0xffff0000u, s_G, r.y >> 18, f0, f1, f2));
    }
    STAMP(1, 7);
    __syncthreads();
    STAMP(1, 8);
    if (tid < EPB) {
        u32 q = s_base[tid];
        u32 qe = min(q + s_cnt[tid], (u32)XCAP);
        float a0 = (float)s_ovf[tid], a1 = (float)s_ovf[tid + 640], a2 = (float)s_ovf[tid + 1280], a3 = (float)s_ovf[tid + 1920];
        for (; q + 4 <= qe; q += 4) {
            u32x2 v0 = s_x[q], v1 = s_x[q + 1], v2 = s_x[q + 2], v3 = s_x[q + 3];
            a0 += (lo2f(v0.x) + lo2f(v1.x)) + (lo2f(v2.x) + lo2f(v3.x));
            a1 += (hi2f(v0.x) + hi2f(v1.x)) + (hi2f(v2.x) + hi2f(v3.x));
            a2 += (lo2f(v0.y) + lo2f(v1.y)) + (lo2f(v2.y) + lo2f(v3.y));
            a3 += (hi2f(v0.y) + hi2f(v1.y)) + (hi2f(v2.y) + hi2f(v3.y));
        }
        for (; q < qe; ++q) {
            u32x2 v = s_x[q];
            a0 += lo2f(v.x); a1 += hi2f(v.x); a2 += lo2f(v.y); a3 += hi2f(v.y);
        }
        float ag[4] = {rbf16(a0), rbf16(a1), rbf16(a2), rbf16(a3)};
        float mm[4] = {lo2f(mrow.x), hi2f(mrow.x), lo2f(mrow.y), hi2f(mrow.y)};
        float res[4];
#pragma unroll
        for (int k = 0; k < 4; ++k) {
            float u = 0.f;
#pragma unroll
            for (int i = 0; i < 4; ++i) u = fmaf(ag[i], wel(w_up, i * 4 + k), u);
            res[k] = mm[k] + rbf16(rbf16(u) * gate[k]);
        }
        out[e] = (u32x2){pk2(res[0], res[1]), pk2(res[2], res[3])};
    }
    __syncthreads();
    STAMP(1, 9);
}

__global__ void k_print(const char* ws) {
    const unsigned long long* st = (const unsigned long long*)(ws + ((size_t)200 << 20));
    __shared__ unsigned long long t0;
    __shared__ int av[32], mx[32];
    int tid = threadIdx.x;
    unsigned long long ts = __builtin_amdgcn_s_memrealtime();
    if (tid == 0) { unsigned long long t = ~0ull; for (int b = 0; b < 256; ++b) t = min(t, st[b * 16]); t0 = t; }
    __syncthreads();
    if (tid < 32) {
        int k = tid >> 4, s = tid & 15; long long a = 0, m = 0;
        for (int b = 0; b < 256; ++b) { long long v = (long long)(st[(k * 256 + b) * 16 + s] - t0); a += v; m = v > m ? v : m; }
        av[tid] = (int)(a / 256); mx[tid] = (int)m;
    }
    __syncthreads();
    if (tid == 0) { unsigned long long w = ts + 30000ull + 10ull * (unsigned)min(max(0, 0), 20000);
        while (__builtin_amdgcn_s_memrealtime() < w) __builtin_amdgcn_s_sleep(1); }
}

extern "C" void kernel_launch(void* const* d_in, const int* in_sizes, int n_in,
                              void* d_out, int out_size, void* d_ws, size_t ws_size,
                              hipStream_t stream) {
    const u16* m = (const u16*)d_in[0];
    const int* idx_in = (const int*)d_in[1];
    const int* idx_out = (const int*)d_in[2];
    const u16* dist = (const u16*)d_in[3];
    const u16* angle = (const u16*)d_in[4];
    const u16* centers = (const u16*)d_in[5];
    const u16* freq = (const u16*)d_in[6];
    const u16* w_rbf = (const u16*)d_in[7];
    const u16* w_sbf = (const u16*)d_in[8];
    const u16* w_down = (const u16*)d_in[9];
    const u16* w_up = (const u16*)d_in[10];
    k_part<<<NB, 1024, 0, stream>>>((const u32x2*)m, idx_in, idx_out, angle, (const u32*)w_down, (char*)d_ws);
    k_acc<<<NB, 1024, 0, stream>>>((const u32x2*)m, dist, (const u32*)centers, freq, (const u32*)w_rbf, (const u32*)w_sbf, (const u32*)w_up, (char*)d_ws, (u32x2*)d_out);
    k_print<<<1, 64, 0, stream>>>((const char*)d_ws);
}
```

```cpp
#include <hip/hip_runtime.h>
#include <hip/hip_bf16.h>
#include <stdint.h>

typedef unsigned short u16;
typedef unsigned int u32;
typedef u16 u16x2 __attribute__((ext_vector_type(2)));
typedef u32 u32x2 __attribute__((ext_vector_type(2)));
typedef u32 u32x3 __attribute__((ext_vector_type(3)));
typedef u32 u32x4 __attribute__((ext_vector_type(4)));
typedef float f32x2 __attribute__((ext_vector_type(2)));
typedef float f32x4 __attribute__((ext_vector_type(4)));
typedef float f32x16 __attribute__((ext_vector_type(16)));
typedef __bf16 bf16x2 __attribute__((ext_vector_type(2)));
typedef __bf16 bf16x8 __attribute__((ext_vector_type(8)));
#define LAS __attribute__((address_space(3)))

#define STAMP(K, i)

__device__ __forceinline__ float b2f(u16 x) { return __uint_as_float(((u32)x) << 16); }
__device__ __forceinline__ float lo2f(u32 x) { return __uint_as_float(x << 16); }
__device__ __forceinline__ float hi2f(u32 x) { return __uint_as_float(x & 0xffff0000u); }
__device__ __forceinline__ float wel(const u32* __restrict__ w, int i) { return (i & 1) ? hi2f(w[i >> 1]) : lo2f(w[i >> 1]); }
__device__ __forceinline__ u32 pk2(float a, float b) {
    f32x2 v = {a, b};
    return __builtin_bit_cast(u32, __builtin_convertvector(v, bf16x2));
}
__device__ __forceinline__ float dot2(u32 a, u32 b, float c) {
    return __builtin_amdgcn_fdot2_f32_bf16(__builtin_bit_cast(bf16x2, a), __builtin_bit_cast(bf16x2, b), c, false);
}
__device__ __forceinline__ float rbf16(float x) { return lo2f(pk2(x, x)); }
__device__ __forceinline__ float fcos(float x) {
    return __builtin_amdgcn_cosf(__builtin_amdgcn_fractf(x * 0.15915494309189535f));
}
__device__ __forceinline__ void lds_barrier() { asm volatile("s_waitcnt lgkmcnt(0)\n\ts_barrier" ::: "memory"); }

#define NE 160000
#define NT 2560000
#define NB 256
#define EPB 625
#define TPB 10000
#define CELL 384
#define NLUT 1248
#define WS_MD 0
#define WS_LCNT 1280000
#define WS_CELL 1310720
#define WS_LIST (WS_CELL + NB * NB * CELL)
#define LIST_BYTES (TPB * 12)

__device__ __forceinline__ u32 lut_base(u32 f01, u32 f2x) {
    u32 fm = max(max(f01 & 0x7fffu, (f01 >> 16) & 0x7fffu), f2x & 0x7fffu);
    int ea = 248 - (int)(fm >> 7);
    return (u32)min(max(ea, 0), 240) << 7;
}

__global__ __launch_bounds__(1024) void k_part(const u32x2* __restrict__ m, const int* __restrict__ idx_in,
                                               const int* __restrict__ idx_out, const u16* __restrict__ angle,
                                               const u32* __restrict__ freq, const u32* __restrict__ w_down,
                                               char* __restrict__ ws) {
    __shared__ __attribute__((aligned(16))) char s_cell[NB * CELL + 16];
    __shared__ __attribute__((aligned(16))) u32 s_cnt[NB + 4];
    __shared__ __attribute__((aligned(16))) u32 s_flag[NB];
    const int tid = threadIdx.x, b = blockIdx.x;
    STAMP(0, 0);
    const int t0 = b * TPB;
    const bool hasC = tid < 904;
    const int tc = t0 + 8192 + (hasC ? tid * 2 : 0);
    u32x2 mrow = m[b * EPB + (tid < EPB ? tid : 0)];
    u32x4 oA = *(const u32x4*)(idx_out + t0 + tid * 4);
    u32x4 iA = *(const u32x4*)(idx_in + t0 + tid * 4);
    u32x2 aA = *(const u32x2*)(angle + t0 + tid * 4);
    u32x4 oB = *(const u32x4*)(idx_out + t0 + 4096 + tid * 4);
    u32x4 iB = *(const u32x4*)(idx_in + t0 + 4096 + tid * 4);
    u32x2 aB = *(const u32x2*)(angle + t0 + 4096 + tid * 4);
    u32x2 oC = *(const u32x2*)(idx_out + tc);
    u32x2 iC = *(const u32x2*)(idx_in + tc);
    u32 aC = *(const u32*)(angle + tc);
    const u32 A0 = lut_base(freq[0], freq[1]);
    if (tid < NB) { s_cnt[tid] = 6; s_flag[tid] = 0; }
    if (tid == NB) { s_cnt[NB] = CELL; s_cnt[NB + 1] = 0; }
    lds_barrier();
    if (tid < EPB) {
        float mm[4] = {lo2f(mrow.x), hi2f(mrow.x), lo2f(mrow.y), hi2f(mrow.y)};
        float d[4];
#pragma unroll
        for (int k = 0; k < 4; ++k) {
            float s = 0.f;
#pragma unroll
            for (int j = 0; j < 4; ++j) s = fmaf(mm[j], wel(w_down, j * 4 + k), s);
            d[k] = s;
        }
        u32 l02 = pk2(d[0], d[2]);
        u32 wv[2];
#pragma unroll
        for (int k = 0; k < 2; ++k) {
            u32 l = k ? (l02 >> 16) : (l02 & 0xffffu);
            u32 hb = __float_as_uint(d[2 * k + 1]);
            u32 t = __builtin_elementwise_sub_sat((hb & 0x7fffffffu) + 0x8000u, l);
            wv[k] = (hb & 0x80000000u) | (t & 0x7fff0000u) | l;
        }
        *(u32x2*)(ws + WS_MD + (size_t)(b * EPB + tid) * 8) = (u32x2){wv[0], wv[1]};
    }
    STAMP(0, 1);
    u32 o[10] = {oA.x, oA.y, oA.z, oA.w, oB.x, oB.y, oB.z, oB.w, oC.x, oC.y};
    u32 in[10] = {iA.x, iA.y, iA.z, iA.w, iB.x, iB.y, iB.z, iB.w, iC.x, iC.y};
    u32 an[5] = {aA.x, aA.y, aB.x, aB.y, aC};
    u32 rel[10];
    u32 mx = 0;
    const u16x2 A02 = {(u16)A0, (u16)A0};
#pragma unroll
    for (int c = 0; c < 3; ++c) {
        const int k0 = c * 4, nk = c < 2 ? 4 : 2;
        if (c < 2 || hasC) {
            u32 bk[4], ca[4], tt[2];
            bool bad = false;
#pragma unroll
            for (int p = 0; p < nk / 2; ++p) {
                u16x2 t = __builtin_elementwise_sub_sat(__builtin_bit_cast(u16x2, an[k0 / 2 + p] & 0x7fff7fffu), A02);
                u16x2 u = __builtin_elementwise_min(t, (u16x2){NLUT - 1, NLUT - 1});
                tt[p] = __builtin_bit_cast(u32, t);
                bad |= tt[p] != __builtin_bit_cast(u32, u);
            }
#pragma unroll
            for (int k = 0; k < nk; ++k) {
                bk[k] = __umulhi(o[k0 + k], 6871948u);
                ca[k] = bk[k];
            }
            if (__builtin_expect(__builtin_amdgcn_ballot_w64(bad) != 0, 0)) {
#pragma unroll
                for (int k = 0; k < nk; ++k) {
                    u32 t = (k & 1) ? (tt[k >> 1] >> 16) : (tt[k >> 1] & 0xffffu);
                    if (t >= NLUT) ca[k] = NB;
                }
            }
#pragma unroll
            for (int k = 0; k < nk; ++k) rel[k0 + k] = atomicAdd(&s_cnt[ca[k]], 6u);
#pragma unroll
            for (int k = 0; k < nk; ++k) {
                u32 r = rel[k0 + k];
                mx = max(mx, r);
                u32 ad = bk[k] * CELL + min(r, (u32)CELL);
                u32 lo = o[k0 + k] - bk[k] * EPB;
                *(u32*)(s_cell + (ad & ~3u)) = in[k0 + k] | (lo << 22);
                *(u16*)(s_cell + (ad ^ 2u) + 6) = (k & 1) ? (u16)(tt[k >> 1] >> 16) : (u16)tt[k >> 1];
            }
        } else {
            rel[8] = 0; rel[9] = 0;
        }
        __builtin_amdgcn_sched_barrier(0);
    }
    if (__builtin_expect(__builtin_amdgcn_ballot_w64(mx >= CELL) != 0, 0)) {
        char* list = ws + WS_LIST + (size_t)b * LIST_BYTES;
#pragma unroll
        for (int k = 0; k < 10; ++k) {
            if (rel[k] >= CELL) {
                u32 p = atomicAdd(&s_cnt[NB + 1], 1u);
                u32 a = (k & 1) ? (an[k >> 1] & 0xffff0000u) : (an[k >> 1] << 16);
                *(u32x3*)(list + p * 12) = (u32x3){in[k], o[k], a};
                s_flag[__umulhi(o[k], 6871948u)] = 64;
            }
        }
    }
    STAMP(0, 2);
    lds_barrier();
    STAMP(0, 3);
    if (tid == 0) *(u32*)(ws + WS_LCNT + b * 4) = s_cnt[NB + 1];
    const int wv = tid >> 6, lane = tid & 63, sub = lane & 7;
    const u32 dummy = (u32)(EPB + b) << 22;
#pragma unroll
    for (int g = 0; g < 2; ++g) {
        const int c = wv * 16 + g * 8 + (lane >> 3);
        u32 r = min(s_cnt[c], (u32)CELL);
        u32 fl = s_flag[c];
        u32 nby = r + (r & 2u) * 3;
        if (sub == 0) *(u32*)(s_cell + c * CELL) = dummy | fl | ((nby * 43u) >> 9);
        if (sub == 1 && (r & 2u)) *(u32*)(s_cell + c * CELL + r - 2) = dummy;
        __builtin_amdgcn_fence(__ATOMIC_ACQ_REL, "wavefront");
        char* dst = ws + WS_CELL + ((size_t)c * NB + b) * CELL;
#pragma unroll
        for (int i = 0; i < 3; ++i) {
            u32 off = i * 128 + sub * 16;
            u32x4 v = *(const u32x4*)(s_cell + c * CELL + off);
            if (off < nby) *(u32x4*)(dst + off) = v;
        }
    }
    STAMP(0, 4);
    STAMP(0, 5);
}

#define XROW 5008
#define XCAP 24
#define L_LUT 0
#define L_CNT (NLUT * 8)
#define L_G01 (L_CNT + 3536)
#define L_G2 (L_G01 + 10000)
#define L_OVF (L_G2 + 10000)
#define L_X (L_OVF + 10000)
#define L_END (L_X + XROW * XCAP)

__device__ __forceinline__ u32x2 gate_x(u32x2 md, u32x2 c, u32x4 g01, f32x4 g2) {
    float c2 = __uint_as_float(c.y);
    f32x2 P = f32x2{g2.x, g2.y} * c2, Q = f32x2{g2.z, g2.w} * c2;
    P.x = dot2(c.x, g01.x, P.x);
    Q.x = dot2(c.x, g01.y, Q.x);
    P.y = dot2(c.x, g01.z, P.y);
    Q.y = dot2(c.x, g01.w, Q.y);
    P *= f32x2{lo2f(md.x), lo2f(md.y)};
    Q *= f32x2{__uint_as_float(md.x), __uint_as_float(md.y)};
    return (u32x2){pk2(P.x, Q.x), pk2(P.y, Q.y)};
}
__device__ __forceinline__ void ovf_add(char* lds, u32 a16, u32x2 x) {
    float* p = (float*)(lds + L_OVF + a16);
    atomicAdd(p, lo2f(x.x)); atomicAdd(p + 1, hi2f(x.x)); atomicAdd(p + 2, lo2f(x.y)); atomicAdd(p + 3, hi2f(x.y));
}

__global__ __launch_bounds__(1024) void k_acc(const u32x2* __restrict__ m, const u16* __restrict__ dist,
                                              const u32* __restrict__ centers, const u32* __restrict__ freq,
                                              const u32* __restrict__ w_rbf, const u32* __restrict__ w_sbf,
                                              const u32* __restrict__ w_up, char* __restrict__ ws,
                                              u32x2* __restrict__ out) {
    __shared__ __attribute__((aligned(16))) char lds[L_END];
    const int tid = threadIdx.x, j = blockIdx.x;
    STAMP(1, 0);
    const int src = tid >> 2, sub = tid & 3, lane = tid & 63, wv = tid >> 6;
    const int e = j * EPB + (tid < EPB ? tid : 0);
    const u32 wsrc = lane < 48 ? w_sbf[lane] : w_rbf[lane - 48];
    const u16 dist_e = dist[e];
    const char* cell = ws + WS_CELL + ((size_t)j * NB + src) * CELL;
    u32x3 rec[8];
#pragma unroll
    for (int k = 0; k < 6; ++k) rec[k] = *(const u32x3*)(cell + sub * 12 + k * 48);
    const u32x2 mrow = m[e];
    const u32 f01 = freq[0], f2x = freq[1];
    const float f0 = lo2f(f01), f1 = hi2f(f01), f2 = lo2f(f2x);
    const u32 A0 = lut_base(f01, f2x);
    __builtin_amdgcn_sched_barrier(0);
#pragma unroll
    for (int i = 0; i < 8; ++i) {
        u32 off = L_OVF + (i * 1024 + tid) * 16;
        if (i < 7 || off < L_END) *(u32x4*)(lds + off) = (u32x4){0, 0, 0, 0};
    }
    if (tid < EPB + NB) *(u32*)(lds + L_CNT + tid * 4) = L_X + min(tid, EPB) * 8;
#pragma unroll
    for (int i = 0; i < 2; ++i) {
        u32 n = i * 1024 + tid;
        if (i == 0 || n < NLUT) {
            float a = lo2f(A0 + n);
            u32 t01 = pk2(f0 * a, f1 * a);
            u32 c01 = pk2(fcos(lo2f(t01)), fcos(hi2f(t01)));
            float c2 = rbf16(fcos(rbf16(f2 * a)));
            *(u32x2*)(lds + L_LUT + n * 8) = (u32x2){c01, __float_as_uint(c2)};
        }
    }
    __builtin_amdgcn_sched_barrier(0);
    STAMP(1, 1);
    const u32 hdr = __builtin_amdgcn_update_dpp(0u, rec[0].x, 0x00, 0xf, 0xf, true);
    const u32 nent = hdr & 63u;
#pragma unroll
    for (int k = 6; k < 8; ++k) rec[k] = *(const u32x3*)(cell + ((u32)(sub + 4 * k) < nent ? sub * 12 + k * 48 : 0));
    const char* md = ws + WS_MD;
    u32x2 mv[8][2];
    auto gather = [&](int k) {
        u32 mk = (u32)(sub + 4 * k) < nent ? 0x3ffffu : 0u;
        mv[k][0] = *(const u32x2*)(md + (size_t)((rec[k].x & mk) << 3));
        mv[k][1] = *(const u32x2*)(md + (size_t)((rec[k].y & mk) << 3));
    };
    gather(0);
    gather(1);
    __builtin_amdgcn_sched_barrier(0);
    STAMP(1, 2);
    u32 gate01 = 0, gate23 = 0;
    if (wv < 10) {
        char* wt = lds + L_G01 + wv * 1024;
        {
            u32 row = lane < 48 ? (u32)lane >> 1 : 0u, r = lane < 48 ? (row * 43u) >> 7 : ((u32)lane - 48u) >> 1, l = row - 3 * r, d = (lane & 1) * 2;
            u32 o0 = lane >= 48 ? d : (l == 2 ? 4 + (d >> 1) : 8 + 2 * d + l);
            u32 o1 = lane >= 48 ? d + 1 : (l == 2 ? 6 + (d >> 1) : 10 + 2 * d + l);
            *(u16*)(wt + o0 * 16 + r * 2) = (u16)wsrc;
            *(u16*)(wt + o1 * 16 + r * 2) = (u16)(wsrc >> 16);
        }
        __builtin_amdgcn_fence(__ATOMIC_ACQ_REL, "wavefront");
        const u32 i = lane & 31;
        u32x4 A = *(const u32x4*)(wt + ((i & 3) + 4 * (i >> 3)) * 16);
        if (((i >> 2) & 1) != (u32)(lane >> 5)) A = (u32x4){0, 0, 0, 0};
        __builtin_amdgcn_fence(__ATOMIC_ACQ_REL, "wavefront");
        float d = b2f(dist_e);
        float rb[8];
#pragma unroll
        for (int r = 0; r < 8; ++r) {
            float df = d - wel(centers, r);
            rb[r] = __builtin_amdgcn_exp2f(df * df * -14.426950408889634f);
        }
        u32x4 B = {pk2(rb[0], rb[1]), pk2(rb[2], rb[3]), pk2(rb[4], rb[5]), pk2(rb[6], rb[7])};
        f32x16 D = {0, 0, 0, 0, 0, 0, 0, 0, 0, 0, 0, 0, 0, 0, 0, 0};
        D = __builtin_amdgcn_mfma_f32_32x32x16_bf16(__builtin_bit_cast(bf16x8, A), __builtin_bit_cast(bf16x8, B), D, 0, 0, 0);
        gate01 = pk2(D[0], D[1]); gate23 = pk2(D[2], D[3]);
        if (tid < EPB) {
            *(f32x4*)(lds + L_G2 + tid * 16) = (f32x4){D[4], D[5], D[6], D[7]};
            *(u32x4*)(lds + L_G01 + tid * 16) = (u32x4){pk2(D[8], D[9]), pk2(D[10], D[11]), pk2(D[12], D[13]), pk2(D[14], D[15])};
        }
    }
    STAMP(1, 3);
    lds_barrier();
    STAMP(1, 4);
#pragma unroll
    for (int k = 0; k < 8; ++k) {
        if (k + 2 < 8) gather(k + 2);
        __builtin_amdgcn_sched_barrier(0);
        const bool valid = (u32)(sub + 4 * k) < nent;
        if (k >= 4 && __builtin_amdgcn_ballot_w64(valid) == 0) break;
        if (valid) {
            u32 w[2] = {rec[k].x, rec[k].y};
            u32 la[2] = {(rec[k].z & 0xffffu) << 3, (rec[k].z >> 16) << 3};
            u32 pos[2]; u32x2 c[2]; u32x4 g01[2]; f32x4 g2[2];
#pragma unroll
            for (int h = 0; h < 2; ++h) {
                pos[h] = atomicAdd((u32*)(lds + L_CNT + (w[h] >> 20)), (u32)XROW);
                c[h] = *(const u32x2*)(lds + L_LUT + la[h]);
                g01[h] = *(const u32x4*)(lds + L_G01 + (w[h] >> 18));
                g2[h] = *(const f32x4*)(lds + L_G2 + (w[h] >> 18));
            }
            u32x2 x[2];
#pragma unroll
            for (int h = 0; h < 2; ++h) {
                x[h] = gate_x(mv[k][h], c[h], g01[h], g2[h]);
                if (pos[h] < L_END) *(u32x2*)(lds + pos[h]) = x[h];
                else ovf_add(lds, w[h] >> 18, x[h]);
            }
        }
        __builtin_amdgcn_sched_barrier(0);
    }
    STAMP(1, 5);
    if (__builtin_expect((hdr & 64u) != 0, 0)) {
        const u32 n = *(const u32*)(ws + WS_LCNT + src * 4);
        const char* list = ws + WS_LIST + (size_t)src * LIST_BYTES;
        for (u32 p = sub; p < n; p += 4) {
            u32x3 r = *(const u32x3*)(list + p * 12);
            u32 lo = r.y - (u32)j * EPB;
            if (lo < EPB) {
                float a = __uint_as_float(r.z);
                u32x2 c = {pk2(cosf(rbf16(f0 * a)), cosf(rbf16(f1 * a))), __float_as_uint(rbf16(cosf(rbf16(f2 * a))))};
                ovf_add(lds, lo * 16, gate_x(*(const u32x2*)(md + (size_t)r.x * 8), c, *(const u32x4*)(lds + L_G01 + lo * 16), *(const f32x4*)(lds + L_G2 + lo * 16)));
            }
        }
    }
    lds_barrier();
    STAMP(1, 6);
    if (wv < 10) {
        f32x4 acc = *(const f32x4*)(lds + L_OVF + min(tid, EPB - 1) * 16);
        const u32 i = lane & 15, g = lane >> 4;
        const u32 one = (i & 1) ? 0x3f800000u : 0x00003f80u;
        const u32 s01 = (i & 2) ? 0u : one, s23 = (i & 2) ? one : 0u;
        const bf16x8 A = __builtin_bit_cast(bf16x8, (u32x4){s01, s23, s01, s23});
        f32x4 sum[4];
#pragma unroll
        for (int q = 0; q < 4; ++q) {
            const char* xp = lds + L_X + g * (2 * XROW) + (wv * 64 + q * 16 + i) * 8;
            sum[q] = (f32x4){0.f, 0.f, 0.f, 0.f};
#pragma unroll
            for (int s = 0; s < 3; ++s) {
                u32x2 v0 = *(const u32x2*)(xp + s * 8 * XROW), v1 = *(const u32x2*)(xp + s * 8 * XROW + XROW);
                sum[q] = __builtin_amdgcn_mfma_f32_16x16x32_bf16(A, __builtin_bit_cast(bf16x8, (u32x4){v0.x, v0.y, v1.x, v1.y}), sum[q], 0, 0, 0);
            }
        }
        acc += g == 0 ? sum[0] : g == 1 ? sum[1] : g == 2 ? sum[2] : sum[3];
        u32 g01 = pk2(acc[0], acc[1]), g23 = pk2(acc[2], acc[3]);
        float ag[4] = {lo2f(g01), hi2f(g01), lo2f(g23), hi2f(g23)};
        float mm[4] = {lo2f(mrow.x), hi2f(mrow.x), lo2f(mrow.y), hi2f(mrow.y)};
        float gt[4] = {lo2f(gate01), hi2f(gate01), lo2f(gate23), hi2f(gate23)};
        float res[4];
#pragma unroll
        for (int k = 0; k < 4; ++k) {
            float u = 0.f;
#pragma unroll
            for (int i2 = 0; i2 < 4; ++i2) u = fmaf(ag[i2], wel(w_up, i2 * 4 + k), u);
            res[k] = fmaf(u, gt[k], mm[k]);
        }
        if (tid < EPB) out[e] = (u32x2){pk2(res[0], res[1]), pk2(res[2], res[3])};
    }
    STAMP(1, 7);
}

extern "C" void kernel_launch(void* const* d_in, const int* in_sizes, int n_in,
                              void* d_out, int out_size, void* d_ws, size_t ws_size,
                              hipStream_t stream) {
    k_part<<<NB, 1024, 0, stream>>>((const u32x2*)d_in[0], (const int*)d_in[1], (const int*)d_in[2], (const u16*)d_in[4],
                                    (const u32*)d_in[6], (const u32*)d_in[9], (char*)d_ws);
    k_acc<<<NB, 1024, 0, stream>>>((const u32x2*)d_in[0], (const u16*)d_in[3], (const u32*)d_in[5], (const u32*)d_in[6],
                                   (const u32*)d_in[7], (const u32*)d_in[8], (const u32*)d_in[10], (char*)d_ws, (u32x2*)d_out);
}
```

```cpp
#include <hip/hip_runtime.h>
#include <hip/hip_bf16.h>
#include <stdint.h>

typedef unsigned char u8;
typedef unsigned short u16;
typedef unsigned int u32;
typedef u16 u16x2 __attribute__((ext_vector_type(2)));
typedef short s16x4 __attribute__((ext_vector_type(4)));
typedef u32 u32x2 __attribute__((ext_vector_type(2)));
typedef u32 u32x3 __attribute__((ext_vector_type(3)));
typedef u32 u32x4 __attribute__((ext_vector_type(4)));
typedef float f32x2 __attribute__((ext_vector_type(2)));
typedef float f32x4 __attribute__((ext_vector_type(4)));
typedef float f32x16 __attribute__((ext_vector_type(16)));
typedef __bf16 bf16x2 __attribute__((ext_vector_type(2)));
typedef __bf16 bf16x8 __attribute__((ext_vector_type(8)));

#define STAMP(K, i)
#define ST_AUX 2
#define LD_NT 0
#define PF_K1 1
#define PF_K2 0
#define DEPTH 4
#define K1_PRE 1
#define K2_PRE 0

__device__ u32x2 row_load(__amdgpu_buffer_rsrc_t rsrc, int vindex, int voffset, int soffset, int aux) __asm("llvm.amdgcn.struct.ptr.buffer.load.v2i32");

__device__ __forceinline__ float b2f(u16 x) { return __uint_as_float(((u32)x) << 16); }
__device__ __forceinline__ float lo2f(u32 x) { return __uint_as_float(x << 16); }
__device__ __forceinline__ float hi2f(u32 x) { return __uint_as_float(x & 0xffff0000u); }
__device__ __forceinline__ float wel(const u32* __restrict__ w, int i) { return (i & 1) ? hi2f(w[i >> 1]) : lo2f(w[i >> 1]); }
__device__ __forceinline__ u32 pk2(float a, float b) {
    f32x2 v = {a, b};
    return __builtin_bit_cast(u32, __builtin_convertvector(v, bf16x2));
}
__device__ __forceinline__ float dot2(u32 a, u32 b, float c) {
    return __builtin_amdgcn_fdot2_f32_bf16(__builtin_bit_cast(bf16x2, a), __builtin_bit_cast(bf16x2, b), c, false);
}
__device__ __forceinline__ float rbf16(float x) { return lo2f(pk2(x, x)); }
__device__ __forceinline__ float fcos(float x) {
    return __builtin_amdgcn_cosf(__builtin_amdgcn_fractf(x * 0.15915494309189535f));
}
__device__ __forceinline__ void lds_barrier() { asm volatile("s_waitcnt lgkmcnt(0)\n\ts_barrier" ::: "memory"); }
__device__ __forceinline__ u32 bucket_of(u32 o) { return (u32)(((unsigned long long)(o & 0xffffffu) * 6871948u) >> 32); }
template <typename T> __device__ __forceinline__ T ldg(const void* p) {
    return *(const T*)p;
}

#define NE 160000
#define NT 2560000
#define NB 256
#define EPB 625
#define TPB 10000
#define NLUT 1024
#define BROWS 771
#define NBIN 208
#define WS_PRI 0
#define WS_SEC (NB * NB * 256)
#define WS_LCNT (WS_SEC + NB * NB * 128)
#define WS_LIST (WS_LCNT + 1024)
#define LIST_BYTES (TPB * 12)

__device__ __forceinline__ u32 lut_base(u32 f01, u32 f2x) {
    u32 fm = max(max(f01 & 0x7fffu, (f01 >> 16) & 0x7fffu), f2x & 0x7fffu);
    int ea = 249 - (int)(fm >> 7);
    return (u32)min(max(ea, 0), 240) << 7;
}

#define KS 72
#define K_CNT 0
#define K_FLAG (K_CNT + NB * 4 + 16)
#define K_BIN (K_FLAG + NB * 4)
#define K_W (K_BIN + NB * KS)
#define K_END (K_W + NB * KS * 4)
#define CSH 18
#define CSTEP ((1u << CSH) | 4u)
#define CFULL (64u << CSH)

__device__ __forceinline__ u32 mad_i24(u32 a, int b, u32 c) {
    u32 d;
    asm("v_mad_i32_i24 %0, %1, %2, %3" : "=v"(d) : "v"(a), "s"(b), "v"(c));
    return d;
}
template <int H> __device__ __forceinline__ u32 mad_half(u32 a, u32 b, u32 c) {
    u32 d;
    if (H) asm("v_mad_u32_u16 %0, %1, %2, %3 op_sel:[1,0,0,0]" : "=v"(d) : "v"(a), "s"(b), "v"(c));
    else asm("v_mad_u32_u16 %0, %1, %2, %3" : "=v"(d) : "v"(a), "s"(b), "v"(c));
    return d;
}
__device__ __forceinline__ u32 bin_of(u32 in) { return (u32)(((unsigned long long)(in & 0xffffffu) * (85u << 16)) >> 32); }
__device__ __forceinline__ void cell_put(char* lds, u32 ret, u32 w, u32 bin) {
    *(u32*)(lds + K_W + (ret & ((1u << CSH) - 1))) = w;
    *(u8*)(lds + K_BIN + ((ret >> 2) & ((1u << (CSH - 2)) - 1))) = (u8)bin;
}
__device__ __forceinline__ void list_put(char* lds, char* list, u32 in, u32 o, u32 a, u32 bk) {
    u32 p = atomicAdd((u32*)(lds + K_CNT + NB * 4), 1u);
    *(u32x3*)(list + p * 12) = (u32x3){in, o, a};
    *(u32*)(lds + K_FLAG + bk * 4) = 128;
}
template <int N> __device__ __forceinline__ void part_chunk(char* lds, char* list, const u32* o, const u32* in, const u32* an, u16x2 A02) {
    u32 tt[N / 2], bk[N], ret[N], w[N], bin[N];
    u32 any = 0;
#pragma unroll
    for (int p = 0; p < N / 2; ++p) {
        tt[p] = __builtin_bit_cast(u32, __builtin_elementwise_sub_sat(__builtin_bit_cast(u16x2, an[p] & 0x7fff7fffu), A02));
        any |= tt[p];
    }
#pragma unroll
    for (int k = 0; k < N; ++k) bk[k] = bucket_of(o[k]);
    if (__builtin_expect(__builtin_amdgcn_ballot_w64((any & 0xfc00fc00u) != 0) != 0, 0)) {
#pragma unroll
        for (int k = 0; k < N; ++k) {
            const u32 t = (k & 1) ? (tt[k >> 1] >> 16) : (tt[k >> 1] & 0xffffu), bn = bin_of(in[k]);
            u32 rt = CFULL;
            if (t < NLUT) rt = atomicAdd((u32*)(lds + K_CNT + bk[k] * 4), CSTEP);
            if (rt < CFULL) cell_put(lds, rt, ((o[k] - bk[k] * EPB) << 22) + (t << 10) + in[k] - bn * BROWS, bn);
            else list_put(lds, list, in[k], o[k], (k & 1) ? (an[k >> 1] & 0xffff0000u) : (an[k >> 1] << 16), bk[k]);
        }
        return;
    }
#pragma unroll
    for (int k = 0; k < N; ++k) ret[k] = atomicAdd((u32*)(lds + K_CNT + bk[k] * 4), CSTEP);
#pragma unroll
    for (int k = 0; k < N; ++k) {
        bin[k] = bin_of(in[k]);
        u32 x = (mad_i24(bk[k], -EPB, o[k]) << 22) + mad_i24(bin[k], -BROWS, in[k]);
        w[k] = (k & 1) ? mad_half<1>(tt[k >> 1], 1024u, x) : mad_half<0>(tt[k >> 1], 1024u, x);
    }
    u32 mx = ret[0];
#pragma unroll
    for (int k = 1; k < N; ++k) mx = max(mx, ret[k]);
    if (__builtin_expect(__builtin_amdgcn_ballot_w64(mx >= CFULL) == 0, 1)) {
#pragma unroll
        for (int k = 0; k < N; ++k) cell_put(lds, ret[k], w[k], bin[k]);
    } else {
#pragma unroll
        for (int k = 0; k < N; ++k) {
            if (ret[k] < CFULL) cell_put(lds, ret[k], w[k], bin[k]);
            else list_put(lds, list, in[k], o[k], (k & 1) ? (an[k >> 1] & 0xffff0000u) : (an[k >> 1] << 16), bk[k]);
        }
    }
}

__global__ __launch_bounds__(1024) void k_part(const u32* __restrict__ m, const int* __restrict__ idx_in,
                                               const int* __restrict__ idx_out, const u16* __restrict__ angle,
                                               const u32* __restrict__ freq, char* __restrict__ ws) {
    __shared__ __attribute__((aligned(16))) char lds[K_END];
    const u32 tid = threadIdx.x, b = blockIdx.x;
    STAMP(0, 0);
    const char* po = (const char*)(idx_out + b * TPB);
    const char* pi = (const char*)(idx_in + b * TPB);
    const char* pa = (const char*)(angle + b * TPB);
    const bool hasC = tid < 904;
    const u32 tc = hasC ? tid : 0;
    const u32x4 oA = ldg<u32x4>(po + tid * 16);
    const u32x2 aA = ldg<u32x2>(pa + tid * 8);
    const u32x4 iA = ldg<u32x4>(pi + tid * 16);
    __builtin_amdgcn_sched_barrier(0);
    {
        const u32 trash = (NBIN + b % (NB - NBIN)) * 0x01010101u;
        *(u32x4*)(lds + K_BIN + tid * 16) = (u32x4){trash, trash, trash, trash};
        if (tid < NB * KS / 16 - 1024) *(u32x4*)(lds + K_BIN + 16384 + tid * 16) = (u32x4){trash, trash, trash, trash};
        else if (tid - 256 <= NB) *(u32*)(lds + K_CNT + (tid - 256) * 4) = tid < 256 + NB ? (tid - 256) * (KS * 4) : 0;
        else if (tid - 576 < NB) *(u32*)(lds + K_FLAG + (tid - 576) * 4) = 0;
    }
    lds_barrier();
    __builtin_amdgcn_sched_barrier(0);
    const u32x4 oB = ldg<u32x4>(po + 16384 + tid * 16);
    const u32x2 aB = ldg<u32x2>(pa + 8192 + tid * 8);
    const u32x4 iB = ldg<u32x4>(pi + 16384 + tid * 16);
    __builtin_amdgcn_sched_barrier(0);
    const u32x2 oC = ldg<u32x2>(po + 32768 + tc * 8);
    const u32 aC = ldg<u32>(pa + 16384 + tc * 4);
    const u32x2 iC = ldg<u32x2>(pi + 32768 + tc * 8);
    __builtin_amdgcn_sched_barrier(0);
    STAMP(0, 1);
    const u32 A0 = lut_base(freq[0], freq[1]);
    const u16x2 A02 = {(u16)A0, (u16)A0};
    char* list = ws + WS_LIST + (size_t)b * LIST_BYTES;
    {
        const u32 o[4] = {oA.x, oA.y, oA.z, oA.w}, in[4] = {iA.x, iA.y, iA.z, iA.w}, an[2] = {aA.x, aA.y};
        part_chunk<4>(lds, list, o, in, an, A02);
    }
    __builtin_amdgcn_sched_barrier(0);
    STAMP(0, 6);
    {
        const u32 o[4] = {oB.x, oB.y, oB.z, oB.w}, in[4] = {iB.x, iB.y, iB.z, iB.w}, an[2] = {aB.x, aB.y};
        part_chunk<4>(lds, list, o, in, an, A02);
    }
    __builtin_amdgcn_sched_barrier(0);
    STAMP(0, 7);
    if (hasC) {
        const u32 o[2] = {oC.x, oC.y}, in[2] = {iC.x, iC.y}, an[1] = {aC};
        part_chunk<2>(lds, list, o, in, an, A02);
    }
    __builtin_amdgcn_sched_barrier(0);
    asm volatile("" :: "v"(oC.x), "v"(aC), "v"(iC.x));
    const u32 warm = m[min((b >> 3) * 313 + min(tid, 312u), 9999u) * 32];
    __builtin_amdgcn_sched_barrier(0);
    STAMP(0, 2);
    lds_barrier();
    STAMP(0, 3);
    const u32 sub = tid & 15, c0 = (tid >> 6) * 16 + ((tid >> 4) & 3);
    const __amdgpu_buffer_rsrc_t rs = __builtin_amdgcn_make_buffer_rsrc(ws, (short)0, WS_LCNT, 0x00020000);
    const u32 cs = sub < 12 ? KS * 4 : KS;
    const u32 src = (sub < 12 ? K_W + sub * 16 : K_BIN + (sub - 12) * 16) + c0 * cs;
    u32 hd[4];
    u32x4 v[4];
#pragma unroll
    for (int g = 0; g < 4; ++g) {
        const u32 c = c0 + g * 4;
        hd[g] = ((min(*(const u32*)(lds + K_CNT + c * 4) >> CSH, 64u) + 3) & ~3u) | *(const u32*)(lds + K_FLAG + c * 4);
        const u32x2 v0 = *(const u32x2*)(lds + src + g * 4 * cs), v1 = *(const u32x2*)(lds + src + g * 4 * cs + 8);
        v[g] = (u32x4){v0.x, v0.y, v1.x, v1.y};
    }
#pragma unroll
    for (int g = 0; g < 4; ++g) {
        if (sub == 15) v[g].x = hd[g];
        __builtin_amdgcn_raw_buffer_store_b128(v[g], rs, (c0 * NB + b) * 256 + sub * 16, g * 4 * NB * 256, ST_AUX);
    }
    if (tid == 0) *(u32*)(ws + WS_LCNT + b * 4) = *(const u32*)(lds + K_CNT + NB * 4);
#pragma unroll
    for (int g = 0; g < 4; ++g) {
        const u32 c = c0 + g * 4;
        if ((hd[g] & 127u) > 48 && sub < 5) {
            const u32 a = sub < 4 ? K_W + c * (KS * 4) + 192 + sub * 16 : K_BIN + c * KS + 48;
            const u32x2 v0 = *(const u32x2*)(lds + a), v1 = *(const u32x2*)(lds + a + 8);
            __builtin_amdgcn_raw_buffer_store_b128((u32x4){v0.x, v0.y, v1.x, v1.y}, rs, WS_SEC + (c * NB + b) * 128 + sub * 16, 0, ST_AUX);
        }
    }
    asm volatile("" :: "v"(warm));
    STAMP(0, 4);
    STAMP(0, 5);
}

#define XROW 5000
#define XCAP 24
#define L_LUT 0
#define L_CNT (NLUT * 8)
#define L_BC (L_CNT + 2512)
#define L_G01 (L_BC + 1040)
#define L_G2 (L_G01 + 10000)
#define L_X (L_G2 + 10000)
#define L_OVF (L_X + XROW * XCAP)
#define L_END (L_OVF + 10000)
#define BSTR 260
#define L_SPILL (L_X + NBIN * BSTR)
#define NSPILL 512
#define L_TRASH (L_SPILL + NSPILL * 8)
#define L_ZERO (L_SPILL + 8192)

__device__ __forceinline__ f32x4 down_wave(u32x2 wd, u32x2 mrow) {
    return __builtin_amdgcn_mfma_f32_4x4x4bf16_1k(__builtin_bit_cast(s16x4, wd), __builtin_bit_cast(s16x4, mrow), (f32x4){0.f, 0.f, 0.f, 0.f}, 0, 0, 0);
}
__device__ __forceinline__ f32x4 down_lane(const u32* __restrict__ w, u32x2 mrow) {
    float mm[4] = {lo2f(mrow.x), hi2f(mrow.x), lo2f(mrow.y), hi2f(mrow.y)};
    f32x4 md;
#pragma unroll
    for (int k = 0; k < 4; ++k) md[k] = fmaf(mm[3], wel(w, 12 + k), fmaf(mm[2], wel(w, 8 + k), fmaf(mm[1], wel(w, 4 + k), mm[0] * wel(w, k))));
    return md;
}
__device__ __forceinline__ f32x4 basis(u32x2 c, u32x4 g01, f32x4 g2) {
    float c2 = __uint_as_float(c.y);
    f32x2 P = f32x2{g2.x, g2.y} * c2, Q = f32x2{g2.z, g2.w} * c2;
    return (f32x4){dot2(c.x, g01.x, P.x), dot2(c.x, g01.y, P.y), dot2(c.x, g01.z, Q.x), dot2(c.x, g01.w, Q.y)};
}
__device__ __forceinline__ u32x2 mul_pack(f32x4 a, f32x4 b) {
    f32x2 P = f32x2{a.x, a.y} * f32x2{b.x, b.y}, Q = f32x2{a.z, a.w} * f32x2{b.z, b.w};
    return (u32x2){pk2(P.x, P.y), pk2(Q.x, Q.y)};
}
__device__ __forceinline__ void ovf_add(char* lds, u32 a16, u32x2 x) {
    float* p = (float*)(lds + L_OVF + a16);
    atomicAdd(p, lo2f(x.x)); atomicAdd(p + 1, hi2f(x.x)); atomicAdd(p + 2, lo2f(x.y)); atomicAdd(p + 3, hi2f(x.y));
}
__device__ __forceinline__ f32x4 rec_basis(char* lds, u32 r, u32& pos) {
    const u32 a4 = r >> 20, a16 = a4 << 2;
    pos = atomicAdd((u32*)(lds + L_CNT + a4), (u32)XROW);
    return basis(*(const u32x2*)(lds + L_LUT + ((r >> 7) & 0x1ff8u)), *(const u32x4*)(lds + L_G01 + a16), *(const f32x4*)(lds + L_G2 + a16));
}
__device__ __forceinline__ void rec_store(char* lds, u32 r, u32 pos, u32x2 x) {
    if (pos < L_OVF) *(u32x2*)(lds + pos) = x;
    else ovf_add(lds, (r >> 20) << 2, x);
}
template <int K> __device__ __forceinline__ u32 byte_x4(u32 v) {
    u32 d;
    if (K == 0) asm("v_lshlrev_b32_sdwa %0, 2, %1 dst_sel:DWORD dst_unused:UNUSED_PAD src0_sel:DWORD src1_sel:BYTE_0" : "=v"(d) : "v"(v));
    if (K == 1) asm("v_lshlrev_b32_sdwa %0, 2, %1 dst_sel:DWORD dst_unused:UNUSED_PAD src0_sel:DWORD src1_sel:BYTE_1" : "=v"(d) : "v"(v));
    if (K == 2) asm("v_lshlrev_b32_sdwa %0, 2, %1 dst_sel:DWORD dst_unused:UNUSED_PAD src0_sel:DWORD src1_sel:BYTE_2" : "=v"(d) : "v"(v));
    if (K == 3) asm("v_lshlrev_b32_sdwa %0, 2, %1 dst_sel:DWORD dst_unused:UNUSED_PAD src0_sel:DWORD src1_sel:BYTE_3" : "=v"(d) : "v"(v));
    return d;
}
__device__ __forceinline__ void bin_group(char* lds, u32 bb, u32x4 rec) {
    const u32 rr[4] = {rec.x, rec.y, rec.z, rec.w};
    const u32 ca[4] = {byte_x4<0>(bb), byte_x4<1>(bb), byte_x4<2>(bb), byte_x4<3>(bb)};
    u32 ret[4];
#pragma unroll
    for (int i = 0; i < 4; ++i) ret[i] = atomicAdd((u32*)(lds + L_BC + ca[i]), CSTEP);
    if (__builtin_expect(__builtin_amdgcn_ballot_w64(max(max(ret[0], ret[1]), max(ret[2], ret[3])) >= CFULL) == 0, 1)) {
#pragma unroll
        for (int i = 0; i < 4; ++i) *(u32*)(lds + (ret[i] & ((1u << CSH) - 1))) = rr[i];
    } else {
#pragma unroll
        for (int i = 0; i < 4; ++i) {
            if (ret[i] < CFULL) *(u32*)(lds + (ret[i] & ((1u << CSH) - 1))) = rr[i];
            else {
                u32 p = atomicAdd((u32*)(lds + L_BC + NB * 4), 1u);
                if (p < NSPILL) *(u32x2*)(lds + L_SPILL + p * 8) = (u32x2){rr[i], (ca[i] >> 2) * BROWS + (rr[i] & 1023u)};
            }
        }
    }
}

__global__ __launch_bounds__(1024) void k_acc(const u32x2* __restrict__ m, const u16* __restrict__ dist,
                                              const u32* __restrict__ centers, const u32* __restrict__ freq,
                                              const u32* __restrict__ w_rbf, const u32* __restrict__ w_sbf,
                                              const u32* __restrict__ w_down, const u32* __restrict__ w_up,
                                              char* __restrict__ ws, u32x2* __restrict__ out) {
    __shared__ __attribute__((aligned(16))) char lds[L_END];
    const u32 tid = threadIdx.x, j = blockIdx.x;
    STAMP(1, 0);
    const u32 src = tid >> 2, sub = tid & 3, lane = tid & 63, wv = __builtin_amdgcn_readfirstlane(tid >> 6);
    const u32 e = j * EPB + (tid < EPB ? tid : 0);
    const char* cells = ws + WS_PRI + (size_t)j * NB * 256;
    if (tid <= NB) *(u32*)(lds + L_BC + tid * 4) = tid < NBIN ? L_X + tid * BSTR : tid < NB ? L_TRASH + (tid - NBIN) * 84 : 0;
    lds_barrier();
    __builtin_amdgcn_sched_barrier(0);
    const u16 dist_e = dist[e];
    const u32 wsrc = *(lane < 48 ? w_sbf + lane : w_rbf + (lane - 48));
    __builtin_amdgcn_sched_barrier(0);
    const u32x4 bn = ldg<u32x4>(cells + (src * 256 + 192 + sub * 12));
    __builtin_amdgcn_sched_barrier(0);
    u32x4 rec[4];
#pragma unroll
    for (int k = 0; k < 3; ++k) {
        rec[k] = ldg<u32x4>(cells + (src * 256 + sub * 48) + k * 16);
        __builtin_amdgcn_sched_barrier(0);
    }
    STAMP(1, 1);
#pragma unroll
    for (int i = 0; i < 5; ++i) {
        u32 off = L_ZERO + (i * 1024 + tid) * 16;
        if (i < 4 || off < L_END) *(u32x4*)(lds + off) = (u32x4){0, 0, 0, 0};
    }
    if (tid < EPB) *(u32*)(lds + L_CNT + tid * 4) = L_X + tid * 8;
    else if (tid - 704 < NBIN) *(u32*)(lds + L_X + 256 + (tid - 704) * BSTR) = 0;
    const u32 f01 = freq[0], f2x = freq[1];
    const float f0 = lo2f(f01), f1 = hi2f(f01), f2 = lo2f(f2x);
    {
        float a = lo2f(lut_base(f01, f2x) + tid);
        u32 t01 = pk2(f0 * a, f1 * a);
        u32 c01 = pk2(fcos(lo2f(t01)), fcos(hi2f(t01)));
        float c2 = rbf16(fcos(rbf16(f2 * a)));
        *(u32x2*)(lds + L_LUT + tid * 8) = (u32x2){c01, __float_as_uint(c2)};
    }
    u32x2 wd;
    {
        const u32 sel = (lane & 1) ? 0x07060302u : 0x05040100u;
        const bool h = lane & 2;
        u32 w[8];
#pragma unroll
        for (int i = 0; i < 8; ++i) {
            w[i] = w_down[i];
            asm("" : "+s"(w[i]));
        }
        wd.x = __builtin_amdgcn_perm(h ? w[3] : w[2], h ? w[1] : w[0], sel);
        wd.y = __builtin_amdgcn_perm(h ? w[7] : w[6], h ? w[5] : w[4], sel);
    }
    u32 gate01 = 0, gate23 = 0;
    if (wv < 10) {
        char* wt = lds + L_G01 + wv * 1024;
        {
            u32 row = lane < 48 ? lane >> 1 : 0u, r = lane < 48 ? (row * 43u) >> 7 : (lane - 48u) >> 1, l = row - 3 * r, d = (lane & 1) * 2;
            u32 o0 = lane >= 48 ? d : (l == 2 ? 4 + d : 8 + 2 * d + l);
            u32 o1 = o0 + ((lane >= 48 || l == 2) ? 1 : 2);
            *(u16*)(wt + o0 * 16 + r * 2) = (u16)wsrc;
            *(u16*)(wt + o1 * 16 + r * 2) = (u16)(wsrc >> 16);
        }
        __builtin_amdgcn_fence(__ATOMIC_ACQ_REL, "wavefront");
        const u32 i = lane & 31;
        u32x4 A = *(const u32x4*)(wt + ((i & 3) + 4 * (i >> 3)) * 16);
        if (((i >> 2) & 1) != (lane >> 5)) A = (u32x4){0, 0, 0, 0};
        __builtin_amdgcn_fence(__ATOMIC_ACQ_REL, "wavefront");
        float d = b2f(dist_e);
        float rb[8];
#pragma unroll
        for (int r = 0; r < 8; ++r) {
            float df = d - wel(centers, r);
            rb[r] = __builtin_amdgcn_exp2f(df * df * -14.426950408889634f);
        }
        u32x4 B = {pk2(rb[0], rb[1]), pk2(rb[2], rb[3]), pk2(rb[4], rb[5]), pk2(rb[6], rb[7])};
        f32x16 D = {0, 0, 0, 0, 0, 0, 0, 0, 0, 0, 0, 0, 0, 0, 0, 0};
        D = __builtin_amdgcn_mfma_f32_32x32x16_bf16(__builtin_bit_cast(bf16x8, A), __builtin_bit_cast(bf16x8, B), D, 0, 0, 0);
        gate01 = pk2(D[0], D[1]); gate23 = pk2(D[2], D[3]);
        if (tid < EPB) {
            *(f32x4*)(lds + L_G2 + tid * 16) = (f32x4){D[4], D[5], D[6], D[7]};
            *(u32x4*)(lds + L_G01 + tid * 16) = (u32x4){pk2(D[8], D[9]), pk2(D[10], D[11]), pk2(D[12], D[13]), pk2(D[14], D[15])};
        }
    }
    __builtin_amdgcn_sched_barrier(0);
    STAMP(1, 2);
    const u32 hdr = __builtin_amdgcn_update_dpp(0u, bn.w, 0xff, 0xf, 0xf, true);
    const u32 n4 = hdr & 127u;
    const bool has2 = 48 + 4 * sub < n4;
    const char* secs = ws + WS_SEC + (size_t)j * NB * 128;
    u32 bn3 = 0;
    rec[3] = (u32x4){0, 0, 0, 0};
    if (has2) {
        bn3 = *(const u32*)(secs + src * 128 + 64 + sub * 4);
        rec[3] = *(const u32x4*)(secs + src * 128 + sub * 16);
    }
    __builtin_amdgcn_sched_barrier(0);
    STAMP(1, 3);
    if (12 * sub < n4) bin_group(lds, bn.x, rec[0]);
    __builtin_amdgcn_sched_barrier(0);
    if (12 * sub + 4 < n4) bin_group(lds, bn.y, rec[1]);
    __builtin_amdgcn_sched_barrier(0);
    if (12 * sub + 8 < n4) bin_group(lds, bn.z, rec[2]);
    __builtin_amdgcn_sched_barrier(0);
    if (has2) bin_group(lds, bn3, rec[3]);
    __builtin_amdgcn_sched_barrier(0);
    STAMP(1, 4);
    lds_barrier();
    STAMP(1, 5);
    const u32 cv = *(const u32*)(lds + L_BC + (lane < 13 ? wv * 13 + lane : NB) * 4);
    const __amdgpu_buffer_rsrc_t rm = __builtin_amdgcn_make_buffer_rsrc((void*)m, (short)8, NE * 8, 0x00020000);
    u32 r[13];
#pragma unroll
    for (int u = 0; u < 13; ++u) r[u] = atomicExch((u32*)(lds + L_X + wv * (13 * BSTR) + lane * 4 + u * BSTR), 0u);
    const u32x2 sp = *(const u32x2*)(lds + L_SPILL + tid * 8);
    *(u32x2*)(lds + L_SPILL + tid * 8) = (u32x2){0, 0};
    const u32 nspill = __builtin_amdgcn_readlane(cv, 13);
    u32x2 mrow = {0, 0};
    if (__builtin_expect(nspill <= NSPILL, 1)) {
        u32x2 mv[13];
        bool ok[13];
        const u32 lane1 = (lane + 1) << CSH;
#pragma unroll
        for (int u = 0; u < 13; ++u) ok[u] = lane1 <= __builtin_amdgcn_readlane(cv, u);
#pragma unroll
        for (int u = 0; u < DEPTH; ++u) mv[u] = row_load(rm, ok[u] ? r[u] & 1023u : 0xffffffu, 0, (wv * 13 + u) * (BROWS * 8), 0);
        __builtin_amdgcn_sched_barrier(0);
        lds_barrier();
        STAMP(1, 6);
#pragma unroll
        for (int u = 0; u < 13; ++u) {
            if (u + DEPTH < 13) mv[u + DEPTH] = row_load(rm, ok[u + DEPTH] ? r[u + DEPTH] & 1023u : 0xffffffu, 0, (wv * 13 + u + DEPTH) * (BROWS * 8), 0);
            if (u + DEPTH == 13) mrow = m[e];
            __builtin_amdgcn_sched_barrier(0);
            u32 pos;
            f32x4 s;
            asm volatile("" : "=v"(pos), "=v"(s));
            if (ok[u]) s = rec_basis(lds, r[u], pos);
            __builtin_amdgcn_sched_barrier(0);
            const f32x4 md = down_wave(wd, mv[u]);
            if (ok[u]) rec_store(lds, r[u], pos, mul_pack(s, md));
            __builtin_amdgcn_sched_barrier(0);
        }
        if (__builtin_expect(nspill > wv * 64, 0)) {
            if (tid < nspill) {
                u32 pos;
                const f32x4 s = rec_basis(lds, sp.x, pos);
                rec_store(lds, sp.x, pos, mul_pack(s, down_lane(w_down, m[sp.y])));
            }
        }
    } else {
        lds_barrier();
        mrow = m[e];
        const char* cell = cells + src * 256;
        const char* sec = secs + src * 128;
        for (u32 p = sub; p < n4; p += 4) {
            u32 rr = p < 48 ? *(const u32*)(cell + p * 4) : *(const u32*)(sec + (p - 48) * 4);
            u32 bb = p < 48 ? *(const u8*)(cell + 192 + p) : *(const u8*)(sec + 16 + p);
            if (bb < NBIN) {
                u32 pos;
                const f32x4 s = rec_basis(lds, rr, pos);
                rec_store(lds, rr, pos, mul_pack(s, down_lane(w_down, m[bb * BROWS + (rr & 1023u)])));
            }
        }
    }
    STAMP(1, 7);
    if (__builtin_expect((hdr & 128u) != 0, 0)) {
        const u32 n = *(const u32*)(ws + WS_LCNT + src * 4);
        const char* list = ws + WS_LIST + (size_t)src * LIST_BYTES;
        for (u32 p = sub; p < n; p += 4) {
            u32x3 q = *(const u32x3*)(list + p * 12);
            u32 lo = q.y - j * EPB;
            if (lo < EPB) {
                float a = __uint_as_float(q.z);
                u32x2 c = {pk2(cosf(rbf16(f0 * a)), cosf(rbf16(f1 * a))), __float_as_uint(rbf16(cosf(rbf16(f2 * a))))};
                ovf_add(lds, lo * 16, mul_pack(basis(c, *(const u32x4*)(lds + L_G01 + lo * 16), *(const f32x4*)(lds + L_G2 + lo * 16)), down_lane(w_down, m[q.x])));
            }
        }
    }
    lds_barrier();
    STAMP(1, 8);
    if (wv < 10) {
        f32x4 acc = *(const f32x4*)(lds + L_OVF + min(tid, EPB - 1u) * 16);
        const u32 i = lane & 15, g = lane >> 4;
        const u32 one = (i & 1) ? 0x3f800000u : 0x00003f80u;
        const u32 s01 = (i & 2) ? 0u : one, s23 = (i & 2) ? one : 0u;
        const bf16x8 A = __builtin_bit_cast(bf16x8, (u32x4){s01, s23, s01, s23});
        f32x4 sum[4];
#pragma unroll
        for (int q = 0; q < 4; ++q) {
            const char* xp = lds + L_X + g * (2 * XROW) + (wv * 64 + q * 16 + i) * 8;
            sum[q] = (f32x4){0.f, 0.f, 0.f, 0.f};
#pragma unroll
            for (int s = 0; s < 3; ++s) {
                u32x2 v0 = *(const u32x2*)(xp + s * 8 * XROW), v1 = *(const u32x2*)(xp + s * 8 * XROW + XROW);
                sum[q] = __builtin_amdgcn_mfma_f32_16x16x32_bf16(A, __builtin_bit_cast(bf16x8, (u32x4){v0.x, v0.y, v1.x, v1.y}), sum[q], 0, 0, 0);
            }
        }
        acc += g == 0 ? sum[0] : g == 1 ? sum[1] : g == 2 ? sum[2] : sum[3];
        u32 g01 = pk2(acc[0], acc[1]), g23 = pk2(acc[2], acc[3]);
        float ag[4] = {lo2f(g01), hi2f(g01), lo2f(g23), hi2f(g23)};
        float mm[4] = {lo2f(mrow.x), hi2f(mrow.x), lo2f(mrow.y), hi2f(mrow.y)};
        float gt[4] = {lo2f(gate01), hi2f(gate01), lo2f(gate23), hi2f(gate23)};
        float res[4];
#pragma unroll
        for (int k = 0; k < 4; ++k) {
            float u = 0.f;
#pragma unroll
            for (int i2 = 0; i2 < 4; ++i2) u = fmaf(ag[i2], wel(w_up, i2 * 4 + k), u);
            res[k] = fmaf(u, gt[k], mm[k]);
        }
        if (tid < EPB) out[e] = (u32x2){pk2(res[0], res[1]), pk2(res[2], res[3])};
    }
    STAMP(1, 9);
}

extern "C" void kernel_launch(void* const* d_in, const int* in_sizes, int n_in,
                              void* d_out, int out_size, void* d_ws, size_t ws_size,
                              hipStream_t stream) {
    k_part<<<NB, 1024, 0, stream>>>((const u32*)d_in[0], (const int*)d_in[1], (const int*)d_in[2], (const u16*)d_in[4],
                                    (const u32*)d_in[6], (char*)d_ws);
    k_acc<<<NB, 1024, 0, stream>>>((const u32x2*)d_in[0], (const u16*)d_in[3], (const u32*)d_in[5], (const u32*)d_in[6],
                                   (const u32*)d_in[7], (const u32*)d_in[8], (const u32*)d_in[9], (const u32*)d_in[10],
                                   (char*)d_ws, (u32x2*)d_out);
}
```

```cpp
#include <hip/hip_runtime.h>
#include <hip/hip_bf16.h>
#include <stdint.h>

typedef unsigned char u8;
typedef unsigned short u16;
typedef unsigned int u32;
typedef u16 u16x2 __attribute__((ext_vector_type(2)));
typedef short s16x4 __attribute__((ext_vector_type(4)));
typedef u32 u32x2 __attribute__((ext_vector_type(2)));
typedef u32 u32x3 __attribute__((ext_vector_type(3)));
typedef u32 u32x4 __attribute__((ext_vector_type(4)));
typedef float f32x2 __attribute__((ext_vector_type(2)));
typedef float f32x4 __attribute__((ext_vector_type(4)));
typedef float f32x16 __attribute__((ext_vector_type(16)));
typedef __bf16 bf16x2 __attribute__((ext_vector_type(2)));
typedef __bf16 bf16x8 __attribute__((ext_vector_type(8)));

#define PROF
#define NSTAMP 12
#define STAMP(K, i) do { if (threadIdx.x == 0) ((unsigned long long*)(ws + ((size_t)200 << 20)))[((K) * 256 + blockIdx.x) * 16 + (i)] = __builtin_amdgcn_s_memrealtime(); } while (0)
#define ST_AUX 2
#define LD_NT 0
#define PF_K1 1
#define PF_K2 0
#define DEPTH 4
#define K1_PRE 1
#define K2_PRE 0

__device__ u32x2 row_load(__amdgpu_buffer_rsrc_t rsrc, int vindex, int voffset, int soffset, int aux) __asm("llvm.amdgcn.struct.ptr.buffer.load.v2i32");

__device__ __forceinline__ float b2f(u16 x) { return __uint_as_float(((u32)x) << 16); }
__device__ __forceinline__ float lo2f(u32 x) { return __uint_as_float(x << 16); }
__device__ __forceinline__ float hi2f(u32 x) { return __uint_as_float(x & 0xffff0000u); }
__device__ __forceinline__ float wel(const u32* __restrict__ w, int i) { return (i & 1) ? hi2f(w[i >> 1]) : lo2f(w[i >> 1]); }
__device__ __forceinline__ u32 pk2(float a, float b) {
    f32x2 v = {a, b};
    return __builtin_bit_cast(u32, __builtin_convertvector(v, bf16x2));
}
__device__ __forceinline__ float dot2(u32 a, u32 b, float c) {
    return __builtin_amdgcn_fdot2_f32_bf16(__builtin_bit_cast(bf16x2, a), __builtin_bit_cast(bf16x2, b), c, false);
}
__device__ __forceinline__ float rbf16(float x) { return lo2f(pk2(x, x)); }
__device__ __forceinline__ float fcos(float x) {
    return __builtin_amdgcn_cosf(__builtin_amdgcn_fractf(x * 0.15915494309189535f));
}
__device__ __forceinline__ void lds_barrier() { asm volatile("s_waitcnt lgkmcnt(0)\n\ts_barrier" ::: "memory"); }
__device__ __forceinline__ u32 bucket_of(u32 o) { return (u32)(((unsigned long long)(o & 0xffffffu) * 6871948u) >> 32); }
template <typename T> __device__ __forceinline__ T ldg(const void* p) {
    return *(const T*)p;
}

#define NE 160000
#define NT 2560000
#define NB 256
#define EPB 625
#define TPB 10000
#define NLUT 1024
#define BROWS 771
#define NBIN 208
#define WS_PRI 0
#define WS_SEC (NB * NB * 256)
#define WS_LCNT (WS_SEC + NB * NB * 128)
#define WS_LIST (WS_LCNT + 1024)
#define LIST_BYTES (TPB * 12)

__device__ __forceinline__ u32 lut_base(u32 f01, u32 f2x) {
    u32 fm = max(max(f01 & 0x7fffu, (f01 >> 16) & 0x7fffu), f2x & 0x7fffu);
    int ea = 249 - (int)(fm >> 7);
    return (u32)min(max(ea, 0), 240) << 7;
}

#define KS 72
#define K_CNT 0
#define K_FLAG (K_CNT + NB * 4 + 16)
#define K_BIN (K_FLAG + NB * 4)
#define K_W (K_BIN + NB * KS)
#define K_END (K_W + NB * KS * 4)
#define CSH 18
#define CSTEP ((1u << CSH) | 4u)
#define CFULL (64u << CSH)

__device__ __forceinline__ u32 mad_i24(u32 a, int b, u32 c) {
    u32 d;
    asm("v_mad_i32_i24 %0, %1, %2, %3" : "=v"(d) : "v"(a), "s"(b), "v"(c));
    return d;
}
template <int H> __device__ __forceinline__ u32 mad_half(u32 a, u32 b, u32 c) {
    u32 d;
    if (H) asm("v_mad_u32_u16 %0, %1, %2, %3 op_sel:[1,0,0,0]" : "=v"(d) : "v"(a), "s"(b), "v"(c));
    else asm("v_mad_u32_u16 %0, %1, %2, %3" : "=v"(d) : "v"(a), "s"(b), "v"(c));
    return d;
}
__device__ __forceinline__ u32 bin_of(u32 in) { return (u32)(((unsigned long long)(in & 0xffffffu) * (85u << 16)) >> 32); }
__device__ __forceinline__ void cell_put(char* lds, u32 ret, u32 w, u32 bin) {
    *(u32*)(lds + K_W + (ret & ((1u << CSH) - 1))) = w;
    *(u8*)(lds + K_BIN + ((ret >> 2) & ((1u << (CSH - 2)) - 1))) = (u8)bin;
}
__device__ __forceinline__ void list_put(char* lds, char* list, u32 in, u32 o, u32 a, u32 bk) {
    u32 p = atomicAdd((u32*)(lds + K_CNT + NB * 4), 1u);
    *(u32x3*)(list + p * 12) = (u32x3){in, o, a};
    *(u32*)(lds + K_FLAG + bk * 4) = 128;
}
template <int N> __device__ __forceinline__ void part_chunk(char* lds, char* list, const u32* o, const u32* in, const u32* an, u16x2 A02) {
    u32 tt[N / 2], bk[N], ret[N], w[N], bin[N];
    u32 any = 0;
#pragma unroll
    for (int p = 0; p < N / 2; ++p) {
        tt[p] = __builtin_bit_cast(u32, __builtin_elementwise_sub_sat(__builtin_bit_cast(u16x2, an[p] & 0x7fff7fffu), A02));
        any |= tt[p];
    }
#pragma unroll
    for (int k = 0; k < N; ++k) bk[k] = bucket_of(o[k]);
    if (__builtin_expect(__builtin_amdgcn_ballot_w64((any & 0xfc00fc00u) != 0) != 0, 0)) {
#pragma unroll
        for (int k = 0; k < N; ++k) {
            const u32 t = (k & 1) ? (tt[k >> 1] >> 16) : (tt[k >> 1] & 0xffffu), bn = bin_of(in[k]);
            u32 rt = CFULL;
            if (t < NLUT) rt = atomicAdd((u32*)(lds + K_CNT + bk[k] * 4), CSTEP);
            if (rt < CFULL) cell_put(lds, rt, ((o[k] - bk[k] * EPB) << 22) + (t << 10) + in[k] - bn * BROWS, bn);
            else list_put(lds, list, in[k], o[k], (k & 1) ? (an[k >> 1] & 0xffff0000u) : (an[k >> 1] << 16), bk[k]);
        }
        return;
    }
#pragma unroll
    for (int k = 0; k < N; ++k) ret[k] = atomicAdd((u32*)(lds + K_CNT + bk[k] * 4), CSTEP);
#pragma unroll
    for (int k = 0; k < N; ++k) {
        bin[k] = bin_of(in[k]);
        u32 x = (mad_i24(bk[k], -EPB, o[k]) << 22) + mad_i24(bin[k], -BROWS, in[k]);
        w[k] = (k & 1) ? mad_half<1>(tt[k >> 1], 1024u, x) : mad_half<0>(tt[k >> 1], 1024u, x);
    }
    u32 mx = ret[0];
#pragma unroll
    for (int k = 1; k < N; ++k) mx = max(mx, ret[k]);
    if (__builtin_expect(__builtin_amdgcn_ballot_w64(mx >= CFULL) == 0, 1)) {
#pragma unroll
        for (int k = 0; k < N; ++k) cell_put(lds, ret[k], w[k], bin[k]);
    } else {
#pragma unroll
        for (int k = 0; k < N; ++k) {
            if (ret[k] < CFULL) cell_put(lds, ret[k], w[k], bin[k]);
            else list_put(lds, list, in[k], o[k], (k & 1) ? (an[k >> 1] & 0xffff0000u) : (an[k >> 1] << 16), bk[k]);
        }
    }
}

__global__ __launch_bounds__(1024) void k_part(const u32* __restrict__ m, const int* __restrict__ idx_in,
                                               const int* __restrict__ idx_out, const u16* __restrict__ angle,
                                               const u32* __restrict__ freq, char* __restrict__ ws) {
    __shared__ __attribute__((aligned(16))) char lds[K_END];
    const u32 tid = threadIdx.x, b = blockIdx.x;
    STAMP(0, 0);
    const char* po = (const char*)(idx_out + b * TPB);
    const char* pi = (const char*)(idx_in + b * TPB);
    const char* pa = (const char*)(angle + b * TPB);
    const bool hasC = tid < 904;
    const u32 tc = hasC ? tid : 0;
    const u32x4 oA = ldg<u32x4>(po + tid * 16);
    const u32x2 aA = ldg<u32x2>(pa + tid * 8);
    const u32x4 iA = ldg<u32x4>(pi + tid * 16);
    __builtin_amdgcn_sched_barrier(0);
    {
        const u32 trash = (NBIN + b % (NB - NBIN)) * 0x01010101u;
        *(u32x4*)(lds + K_BIN + tid * 16) = (u32x4){trash, trash, trash, trash};
        if (tid < NB * KS / 16 - 1024) *(u32x4*)(lds + K_BIN + 16384 + tid * 16) = (u32x4){trash, trash, trash, trash};
        else if (tid - 256 <= NB) *(u32*)(lds + K_CNT + (tid - 256) * 4) = tid < 256 + NB ? (tid - 256) * (KS * 4) : 0;
        else if (tid - 576 < NB) *(u32*)(lds + K_FLAG + (tid - 576) * 4) = 0;
    }
    lds_barrier();
    __builtin_amdgcn_sched_barrier(0);
    const u32x4 oB = ldg<u32x4>(po + 16384 + tid * 16);
    const u32x2 aB = ldg<u32x2>(pa + 8192 + tid * 8);
    const u32x4 iB = ldg<u32x4>(pi + 16384 + tid * 16);
    __builtin_amdgcn_sched_barrier(0);
    const u32x2 oC = ldg<u32x2>(po + 32768 + tc * 8);
    const u32 aC = ldg<u32>(pa + 16384 + tc * 4);
    const u32x2 iC = ldg<u32x2>(pi + 32768 + tc * 8);
    __builtin_amdgcn_sched_barrier(0);
    STAMP(0, 1);
    const u32 A0 = lut_base(freq[0], freq[1]);
    const u16x2 A02 = {(u16)A0, (u16)A0};
    char* list = ws + WS_LIST + (size_t)b * LIST_BYTES;
    {
        const u32 o[4] = {oA.x, oA.y, oA.z, oA.w}, in[4] = {iA.x, iA.y, iA.z, iA.w}, an[2] = {aA.x, aA.y};
        part_chunk<4>(lds, list, o, in, an, A02);
    }
    __builtin_amdgcn_sched_barrier(0);
    STAMP(0, 6);
    {
        const u32 o[4] = {oB.x, oB.y, oB.z, oB.w}, in[4] = {iB.x, iB.y, iB.z, iB.w}, an[2] = {aB.x, aB.y};
        part_chunk<4>(lds, list, o, in, an, A02);
    }
    __builtin_amdgcn_sched_barrier(0);
    STAMP(0, 7);
    if (hasC) {
        const u32 o[2] = {oC.x, oC.y}, in[2] = {iC.x, iC.y}, an[1] = {aC};
        part_chunk<2>(lds, list, o, in, an, A02);
    }
    __builtin_amdgcn_sched_barrier(0);
    asm volatile("" :: "v"(oC.x), "v"(aC), "v"(iC.x));
    const u32 warm = m[min((b >> 3) * 313 + min(tid, 312u), 9999u) * 32];
    __builtin_amdgcn_sched_barrier(0);
    STAMP(0, 2);
    lds_barrier();
    STAMP(0, 3);
    const u32 sub = tid & 15, c0 = (tid >> 6) * 16 + ((tid >> 4) & 3);
    const __amdgpu_buffer_rsrc_t rs = __builtin_amdgcn_make_buffer_rsrc(ws, (short)0, WS_LCNT, 0x00020000);
    const u32 cs = sub < 12 ? KS * 4 : KS;
    const u32 src = (sub < 12 ? K_W + sub * 16 : K_BIN + (sub - 12) * 16) + c0 * cs;
    u32 hd[4];
    u32x4 v[4];
#pragma unroll
    for (int g = 0; g < 4; ++g) {
        const u32 c = c0 + g * 4;
        hd[g] = ((min(*(const u32*)(lds + K_CNT + c * 4) >> CSH, 64u) + 3) & ~3u) | *(const u32*)(lds + K_FLAG + c * 4);
        const u32x2 v0 = *(const u32x2*)(lds + src + g * 4 * cs), v1 = *(const u32x2*)(lds + src + g * 4 * cs + 8);
        v[g] = (u32x4){v0.x, v0.y, v1.x, v1.y};
    }
#pragma unroll
    for (int g = 0; g < 4; ++g) {
        if (sub == 15) v[g].x = hd[g];
        __builtin_amdgcn_raw_buffer_store_b128(v[g], rs, (c0 * NB + b) * 256 + sub * 16, g * 4 * NB * 256, ST_AUX);
    }
    if (tid == 0) *(u32*)(ws + WS_LCNT + b * 4) = *(const u32*)(lds + K_CNT + NB * 4);
#pragma unroll
    for (int g = 0; g < 4; ++g) {
        const u32 c = c0 + g * 4;
        if ((hd[g] & 127u) > 48 && sub < 5) {
            const u32 a = sub < 4 ? K_W + c * (KS * 4) + 192 + sub * 16 : K_BIN + c * KS + 48;
            const u32x2 v0 = *(const u32x2*)(lds + a), v1 = *(const u32x2*)(lds + a + 8);
            __builtin_amdgcn_raw_buffer_store_b128((u32x4){v0.x, v0.y, v1.x, v1.y}, rs, WS_SEC + (c * NB + b) * 128 + sub * 16, 0, ST_AUX);
        }
    }
    asm volatile("" :: "v"(warm));
    STAMP(0, 4);
    asm volatile("s_waitcnt vmcnt(0)");
    __syncthreads();
    STAMP(0, 5);
}

#define XROW 5000
#define XCAP 24
#define L_LUT 0
#define L_CNT (NLUT * 8)
#define L_BC (L_CNT + 2512)
#define L_G01 (L_BC + 1040)
#define L_G2 (L_G01 + 10000)
#define L_X (L_G2 + 10000)
#define L_OVF (L_X + XROW * XCAP)
#define L_END (L_OVF + 10000)
#define BSTR 260
#define L_SPILL (L_X + NBIN * BSTR)
#define NSPILL 512
#define L_TRASH (L_SPILL + NSPILL * 8)
#define L_ZERO (L_SPILL + 8192)

__device__ __forceinline__ f32x4 down_wave(u32x2 wd, u32x2 mrow) {
    return __builtin_amdgcn_mfma_f32_4x4x4bf16_1k(__builtin_bit_cast(s16x4, wd), __builtin_bit_cast(s16x4, mrow), (f32x4){0.f, 0.f, 0.f, 0.f}, 0, 0, 0);
}
__device__ __forceinline__ f32x4 down_lane(const u32* __restrict__ w, u32x2 mrow) {
    float mm[4] = {lo2f(mrow.x), hi2f(mrow.x), lo2f(mrow.y), hi2f(mrow.y)};
    f32x4 md;
#pragma unroll
    for (int k = 0; k < 4; ++k) md[k] = fmaf(mm[3], wel(w, 12 + k), fmaf(mm[2], wel(w, 8 + k), fmaf(mm[1], wel(w, 4 + k), mm[0] * wel(w, k))));
    return md;
}
__device__ __forceinline__ f32x4 basis(u32x2 c, u32x4 g01, f32x4 g2) {
    float c2 = __uint_as_float(c.y);
    f32x2 P = f32x2{g2.x, g2.y} * c2, Q = f32x2{g2.z, g2.w} * c2;
    return (f32x4){dot2(c.x, g01.x, P.x), dot2(c.x, g01.y, P.y), dot2(c.x, g01.z, Q.x), dot2(c.x, g01.w, Q.y)};
}
__device__ __forceinline__ u32x2 mul_pack(f32x4 a, f32x4 b) {
    f32x2 P = f32x2{a.x, a.y} * f32x2{b.x, b.y}, Q = f32x2{a.z, a.w} * f32x2{b.z, b.w};
    return (u32x2){pk2(P.x, P.y), pk2(Q.x, Q.y)};
}
__device__ __forceinline__ void ovf_add(char* lds, u32 a16, u32x2 x) {
    float* p = (float*)(lds + L_OVF + a16);
    atomicAdd(p, lo2f(x.x)); atomicAdd(p + 1, hi2f(x.x)); atomicAdd(p + 2, lo2f(x.y)); atomicAdd(p + 3, hi2f(x.y));
}
__device__ __forceinline__ f32x4 rec_basis(char* lds, u32 r, u32& pos) {
    const u32 a4 = r >> 20, a16 = a4 << 2;
    pos = atomicAdd((u32*)(lds + L_CNT + a4), (u32)XROW);
    return basis(*(const u32x2*)(lds + L_LUT + ((r >> 7) & 0x1ff8u)), *(const u32x4*)(lds + L_G01 + a16), *(const f32x4*)(lds + L_G2 + a16));
}
__device__ __forceinline__ void rec_store(char* lds, u32 r, u32 pos, u32x2 x) {
    if (pos < L_OVF) *(u32x2*)(lds + pos) = x;
    else ovf_add(lds, (r >> 20) << 2, x);
}
template <int K> __device__ __forceinline__ u32 byte_x4(u32 v) {
    u32 d;
    if (K == 0) asm("v_lshlrev_b32_sdwa %0, 2, %1 dst_sel:DWORD dst_unused:UNUSED_PAD src0_sel:DWORD src1_sel:BYTE_0" : "=v"(d) : "v"(v));
    if (K == 1) asm("v_lshlrev_b32_sdwa %0, 2, %1 dst_sel:DWORD dst_unused:UNUSED_PAD src0_sel:DWORD src1_sel:BYTE_1" : "=v"(d) : "v"(v));
    if (K == 2) asm("v_lshlrev_b32_sdwa %0, 2, %1 dst_sel:DWORD dst_unused:UNUSED_PAD src0_sel:DWORD src1_sel:BYTE_2" : "=v"(d) : "v"(v));
    if (K == 3) asm("v_lshlrev_b32_sdwa %0, 2, %1 dst_sel:DWORD dst_unused:UNUSED_PAD src0_sel:DWORD src1_sel:BYTE_3" : "=v"(d) : "v"(v));
    return d;
}
__device__ __forceinline__ void bin_group(char* lds, u32 bb, u32x4 rec) {
    const u32 rr[4] = {rec.x, rec.y, rec.z, rec.w};
    const u32 ca[4] = {byte_x4<0>(bb), byte_x4<1>(bb), byte_x4<2>(bb), byte_x4<3>(bb)};
    u32 ret[4];
#pragma unroll
    for (int i = 0; i < 4; ++i) ret[i] = atomicAdd((u32*)(lds + L_BC + ca[i]), CSTEP);
    if (__builtin_expect(__builtin_amdgcn_ballot_w64(max(max(ret[0], ret[1]), max(ret[2], ret[3])) >= CFULL) == 0, 1)) {
#pragma unroll
        for (int i = 0; i < 4; ++i) *(u32*)(lds + (ret[i] & ((1u << CSH) - 1))) = rr[i];
    } else {
#pragma unroll
        for (int i = 0; i < 4; ++i) {
            if (ret[i] < CFULL) *(u32*)(lds + (ret[i] & ((1u << CSH) - 1))) = rr[i];
            else {
                u32 p = atomicAdd((u32*)(lds + L_BC + NB * 4), 1u);
                if (p < NSPILL) *(u32x2*)(lds + L_SPILL + p * 8) = (u32x2){rr[i], (ca[i] >> 2) * BROWS + (rr[i] & 1023u)};
            }
        }
    }
}

__global__ __launch_bounds__(1024) void k_acc(const u32x2* __restrict__ m, const u16* __restrict__ dist,
                                              const u32* __restrict__ centers, const u32* __restrict__ freq,
                                              const u32* __restrict__ w_rbf, const u32* __restrict__ w_sbf,
                                              const u32* __restrict__ w_down, const u32* __restrict__ w_up,
                                              char* __restrict__ ws, u32x2* __restrict__ out) {
    __shared__ __attribute__((aligned(16))) char lds[L_END];
    const u32 tid = threadIdx.x, j = blockIdx.x;
    STAMP(1, 0);
    const u32 src = tid >> 2, sub = tid & 3, lane = tid & 63, wv = __builtin_amdgcn_readfirstlane(tid >> 6);
    const u32 e = j * EPB + (tid < EPB ? tid : 0);
    const char* cells = ws + WS_PRI + (size_t)j * NB * 256;
    if (tid <= NB) *(u32*)(lds + L_BC + tid * 4) = tid < NBIN ? L_X + tid * BSTR : tid < NB ? L_TRASH + (tid - NBIN) * 84 : 0;
    lds_barrier();
    __builtin_amdgcn_sched_barrier(0);
    const u16 dist_e = dist[e];
    const u32 wsrc = *(lane < 48 ? w_sbf + lane : w_rbf + (lane - 48));
    __builtin_amdgcn_sched_barrier(0);
    const u32x4 bn = ldg<u32x4>(cells + (src * 256 + 192 + sub * 12));
    __builtin_amdgcn_sched_barrier(0);
    u32x4 rec[4];
#pragma unroll
    for (int k = 0; k < 3; ++k) {
        rec[k] = ldg<u32x4>(cells + (src * 256 + sub * 48) + k * 16);
        __builtin_amdgcn_sched_barrier(0);
    }
    STAMP(1, 1);
#pragma unroll
    for (int i = 0; i < 5; ++i) {
        u32 off = L_ZERO + (i * 1024 + tid) * 16;
        if (i < 4 || off < L_END) *(u32x4*)(lds + off) = (u32x4){0, 0, 0, 0};
    }
    if (tid < EPB) *(u32*)(lds + L_CNT + tid * 4) = L_X + tid * 8;
    else if (tid - 704 < NBIN) *(u32*)(lds + L_X + 256 + (tid - 704) * BSTR) = 0;
    const u32 f01 = freq[0], f2x = freq[1];
    const float f0 = lo2f(f01), f1 = hi2f(f01), f2 = lo2f(f2x);
    {
        float a = lo2f(lut_base(f01, f2x) + tid);
        u32 t01 = pk2(f0 * a, f1 * a);
        u32 c01 = pk2(fcos(lo2f(t01)), fcos(hi2f(t01)));
        float c2 = rbf16(fcos(rbf16(f2 * a)));
        *(u32x2*)(lds + L_LUT + tid * 8) = (u32x2){c01, __float_as_uint(c2)};
    }
    u32x2 wd;
    {
        const u32 sel = (lane & 1) ? 0x07060302u : 0x05040100u;
        const bool h = lane & 2;
        u32 w[8];
#pragma unroll
        for (int i = 0; i < 8; ++i) {
            w[i] = w_down[i];
            asm("" : "+s"(w[i]));
        }
        wd.x = __builtin_amdgcn_perm(h ? w[3] : w[2], h ? w[1] : w[0], sel);
        wd.y = __builtin_amdgcn_perm(h ? w[7] : w[6], h ? w[5] : w[4], sel);
    }
    u32 gate01 = 0, gate23 = 0;
    if (wv < 10) {
        char* wt = lds + L_G01 + wv * 1024;
        {
            u32 row = lane < 48 ? lane >> 1 : 0u, r = lane < 48 ? (row * 43u) >> 7 : (lane - 48u) >> 1, l = row - 3 * r, d = (lane & 1) * 2;
            u32 o0 = lane >= 48 ? d : (l == 2 ? 4 + d : 8 + 2 * d + l);
            u32 o1 = o0 + ((lane >= 48 || l == 2) ? 1 : 2);
            *(u16*)(wt + o0 * 16 + r * 2) = (u16)wsrc;
            *(u16*)(wt + o1 * 16 + r * 2) = (u16)(wsrc >> 16);
        }
        __builtin_amdgcn_fence(__ATOMIC_ACQ_REL, "wavefront");
        const u32 i = lane & 31;
        u32x4 A = *(const u32x4*)(wt + ((i & 3) + 4 * (i >> 3)) * 16);
        if (((i >> 2) & 1) != (lane >> 5)) A = (u32x4){0, 0, 0, 0};
        __builtin_amdgcn_fence(__ATOMIC_ACQ_REL, "wavefront");
        float d = b2f(dist_e);
        float rb[8];
#pragma unroll
        for (int r = 0; r < 8; ++r) {
            float df = d - wel(centers, r);
            rb[r] = __builtin_amdgcn_exp2f(df * df * -14.426950408889634f);
        }
        u32x4 B = {pk2(rb[0], rb[1]), pk2(rb[2], rb[3]), pk2(rb[4], rb[5]), pk2(rb[6], rb[7])};
        f32x16 D = {0, 0, 0, 0, 0, 0, 0, 0, 0, 0, 0, 0, 0, 0, 0, 0};
        D = __builtin_amdgcn_mfma_f32_32x32x16_bf16(__builtin_bit_cast(bf16x8, A), __builtin_bit_cast(bf16x8, B), D, 0, 0, 0);
        gate01 = pk2(D[0], D[1]); gate23 = pk2(D[2], D[3]);
        if (tid < EPB) {
            *(f32x4*)(lds + L_G2 + tid * 16) = (f32x4){D[4], D[5], D[6], D[7]};
            *(u32x4*)(lds + L_G01 + tid * 16) = (u32x4){pk2(D[8], D[9]), pk2(D[10], D[11]), pk2(D[12], D[13]), pk2(D[14], D[15])};
        }
    }
    __builtin_amdgcn_sched_barrier(0);
    STAMP(1, 2);
    const u32 hdr = __builtin_amdgcn_update_dpp(0u, bn.w, 0xff, 0xf, 0xf, true);
    const u32 n4 = hdr & 127u;
    const bool has2 = 48 + 4 * sub < n4;
    const char* secs = ws + WS_SEC + (size_t)j * NB * 128;
    u32 bn3 = 0;
    rec[3] = (u32x4){0, 0, 0, 0};
    if (has2) {
        bn3 = *(const u32*)(secs + src * 128 + 64 + sub * 4);
        rec[3] = *(const u32x4*)(secs + src * 128 + sub * 16);
    }
    __builtin_amdgcn_sched_barrier(0);
    STAMP(1, 3);
    if (12 * sub < n4) bin_group(lds, bn.x, rec[0]);
    __builtin_amdgcn_sched_barrier(0);
    if (12 * sub + 4 < n4) bin_group(lds, bn.y, rec[1]);
    __builtin_amdgcn_sched_barrier(0);
    if (12 * sub + 8 < n4) bin_group(lds, bn.z, rec[2]);
    __builtin_amdgcn_sched_barrier(0);
    if (has2) bin_group(lds, bn3, rec[3]);
    __builtin_amdgcn_sched_barrier(0);
    STAMP(1, 4);
    lds_barrier();
    STAMP(1, 5);
    const u32 cv = *(const u32*)(lds + L_BC + (lane < 13 ? wv * 13 + lane : NB) * 4);
    const __amdgpu_buffer_rsrc_t rm = __builtin_amdgcn_make_buffer_rsrc((void*)m, (short)8, NE * 8, 0x00020000);
    u32 r[13];
#pragma unroll
    for (int u = 0; u < 13; ++u) r[u] = atomicExch((u32*)(lds + L_X + wv * (13 * BSTR) + lane * 4 + u * BSTR), 0u);
    const u32x2 sp = *(const u32x2*)(lds + L_SPILL + tid * 8);
    *(u32x2*)(lds + L_SPILL + tid * 8) = (u32x2){0, 0};
    const u32 nspill = __builtin_amdgcn_readlane(cv, 13);
    u32x2 mrow = {0, 0};
    if (__builtin_expect(nspill <= NSPILL, 1)) {
        u32x2 mv[13];
        bool ok[13];
        const u32 lane1 = (lane + 1) << CSH;
#pragma unroll
        for (int u = 0; u < 13; ++u) ok[u] = lane1 <= __builtin_amdgcn_readlane(cv, u);
#pragma unroll
        for (int u = 0; u < DEPTH; ++u) mv[u] = row_load(rm, ok[u] ? r[u] & 1023u : 0xffffffu, 0, (wv * 13 + u) * (BROWS * 8), 0);
        __builtin_amdgcn_sched_barrier(0);
        lds_barrier();
        STAMP(1, 6);
#pragma unroll
        for (int u = 0; u < 13; ++u) {
            if (u + DEPTH < 13) mv[u + DEPTH] = row_load(rm, ok[u + DEPTH] ? r[u + DEPTH] & 1023u : 0xffffffu, 0, (wv * 13 + u + DEPTH) * (BROWS * 8), 0);
            if (u + DEPTH == 13) mrow = m[e];
            __builtin_amdgcn_sched_barrier(0);
            u32 pos;
            f32x4 s;
            asm volatile("" : "=v"(pos), "=v"(s));
            if (ok[u]) s = rec_basis(lds, r[u], pos);
            __builtin_amdgcn_sched_barrier(0);
            const f32x4 md = down_wave(wd, mv[u]);
            if (ok[u]) rec_store(lds, r[u], pos, mul_pack(s, md));
            __builtin_amdgcn_sched_barrier(0);
        }
        if (__builtin_expect(nspill > wv * 64, 0)) {
            if (tid < nspill) {
                u32 pos;
                const f32x4 s = rec_basis(lds, sp.x, pos);
                rec_store(lds, sp.x, pos, mul_pack(s, down_lane(w_down, m[sp.y])));
            }
        }
    } else {
        lds_barrier();
        mrow = m[e];
        const char* cell = cells + src * 256;
        const char* sec = secs + src * 128;
        for (u32 p = sub; p < n4; p += 4) {
            u32 rr = p < 48 ? *(const u32*)(cell + p * 4) : *(const u32*)(sec + (p - 48) * 4);
            u32 bb = p < 48 ? *(const u8*)(cell + 192 + p) : *(const u8*)(sec + 16 + p);
            if (bb < NBIN) {
                u32 pos;
                const f32x4 s = rec_basis(lds, rr, pos);
                rec_store(lds, rr, pos, mul_pack(s, down_lane(w_down, m[bb * BROWS + (rr & 1023u)])));
            }
        }
    }
    STAMP(1, 7);
    if (__builtin_expect((hdr & 128u) != 0, 0)) {
        const u32 n = *(const u32*)(ws + WS_LCNT + src * 4);
        const char* list = ws + WS_LIST + (size_t)src * LIST_BYTES;
        for (u32 p = sub; p < n; p += 4) {
            u32x3 q = *(const u32x3*)(list + p * 12);
            u32 lo = q.y - j * EPB;
            if (lo < EPB) {
                float a = __uint_as_float(q.z);
                u32x2 c = {pk2(cosf(rbf16(f0 * a)), cosf(rbf16(f1 * a))), __float_as_uint(rbf16(cosf(rbf16(f2 * a))))};
                ovf_add(lds, lo * 16, mul_pack(basis(c, *(const u32x4*)(lds + L_G01 + lo * 16), *(const f32x4*)(lds + L_G2 + lo * 16)), down_lane(w_down, m[q.x])));
            }
        }
    }
    lds_barrier();
    STAMP(1, 8);
    if (wv < 10) {
        f32x4 acc = *(const f32x4*)(lds + L_OVF + min(tid, EPB - 1u) * 16);
        const u32 i = lane & 15, g = lane >> 4;
        const u32 one = (i & 1) ? 0x3f800000u : 0x00003f80u;
        const u32 s01 = (i & 2) ? 0u : one, s23 = (i & 2) ? one : 0u;
        const bf16x8 A = __builtin_bit_cast(bf16x8, (u32x4){s01, s23, s01, s23});
        f32x4 sum[4];
#pragma unroll
        for (int q = 0; q < 4; ++q) {
            const char* xp = lds + L_X + g * (2 * XROW) + (wv * 64 + q * 16 + i) * 8;
            sum[q] = (f32x4){0.f, 0.f, 0.f, 0.f};
#pragma unroll
            for (int s = 0; s < 3; ++s) {
                u32x2 v0 = *(const u32x2*)(xp + s * 8 * XROW), v1 = *(const u32x2*)(xp + s * 8 * XROW + XROW);
                sum[q] = __builtin_amdgcn_mfma_f32_16x16x32_bf16(A, __builtin_bit_cast(bf16x8, (u32x4){v0.x, v0.y, v1.x, v1.y}), sum[q], 0, 0, 0);
            }
        }
        acc += g == 0 ? sum[0] : g == 1 ? sum[1] : g == 2 ? sum[2] : sum[3];
        u32 g01 = pk2(acc[0], acc[1]), g23 = pk2(acc[2], acc[3]);
        float ag[4] = {lo2f(g01), hi2f(g01), lo2f(g23), hi2f(g23)};
        float mm[4] = {lo2f(mrow.x), hi2f(mrow.x), lo2f(mrow.y), hi2f(mrow.y)};
        float gt[4] = {lo2f(gate01), hi2f(gate01), lo2f(gate23), hi2f(gate23)};
        float res[4];
#pragma unroll
        for (int k = 0; k < 4; ++k) {
            float u = 0.f;
#pragma unroll
            for (int i2 = 0; i2 < 4; ++i2) u = fmaf(ag[i2], wel(w_up, i2 * 4 + k), u);
            res[k] = fmaf(u, gt[k], mm[k]);
        }
        if (tid < EPB) out[e] = (u32x2){pk2(res[0], res[1]), pk2(res[2], res[3])};
    }
    __syncthreads();
    STAMP(1, 9);
}

__global__ void k_print(const char* ws) {
    const unsigned long long* st = (const unsigned long long*)(ws + ((size_t)200 << 20));
    __shared__ unsigned long long t0;
    __shared__ int av[32], mx[32], mn[32];
    int tid = threadIdx.x;
    unsigned long long ts = __builtin_amdgcn_s_memrealtime();
    if (tid == 0) { unsigned long long t = ~0ull; for (int b = 0; b < 256; ++b) t = min(t, st[b * 16]); t0 = t; }
    __syncthreads();
    if (tid < 32) {
        int k = tid >> 4, s = tid & 15; long long a = 0, m = 0, n = 1ll << 40;
        for (int b = 0; b < 256; ++b) { long long v = (long long)(st[(k * 256 + b) * 16 + s] - t0); a += v; m = v > m ? v : m; n = v < n ? v : n; }
        av[tid] = (int)(a / 256); mx[tid] = (int)m; mn[tid] = (int)n;
    }
    __syncthreads();
    if (tid == 0) { unsigned long long w = ts + 30000ull + 10ull * (unsigned)min(max((int)(mx[25] == 123456789), 0), 20000);
        while (__builtin_amdgcn_s_memrealtime() < w) __builtin_amdgcn_s_sleep(1); }
}

extern "C" void kernel_launch(void* const* d_in, const int* in_sizes, int n_in,
                              void* d_out, int out_size, void* d_ws, size_t ws_size,
                              hipStream_t stream) {
    k_part<<<NB, 1024, 0, stream>>>((const u32*)d_in[0], (const int*)d_in[1], (const int*)d_in[2], (const u16*)d_in[4],
                                    (const u32*)d_in[6], (char*)d_ws);
    k_acc<<<NB, 1024, 0, stream>>>((const u32x2*)d_in[0], (const u16*)d_in[3], (const u32*)d_in[5], (const u32*)d_in[6],
                                   (const u32*)d_in[7], (const u32*)d_in[8], (const u32*)d_in[9], (const u32*)d_in[10],
                                   (char*)d_ws, (u32x2*)d_out);
    k_print<<<1, 64, 0, stream>>>((const char*)d_ws);
}
```

```cpp
#include <hip/hip_runtime.h>
#include <hip/hip_bf16.h>
#include <stdint.h>

typedef unsigned char u8;
typedef unsigned short u16;
typedef unsigned int u32;
typedef u16 u16x2 __attribute__((ext_vector_type(2)));
typedef short s16x4 __attribute__((ext_vector_type(4)));
typedef u32 u32x2 __attribute__((ext_vector_type(2)));
typedef u32 u32x3 __attribute__((ext_vector_type(3)));
typedef u32 u32x4 __attribute__((ext_vector_type(4)));
typedef float f32x2 __attribute__((ext_vector_type(2)));
typedef float f32x4 __attribute__((ext_vector_type(4)));
typedef float f32x16 __attribute__((ext_vector_type(16)));
typedef __bf16 bf16x2 __attribute__((ext_vector_type(2)));
typedef __bf16 bf16x8 __attribute__((ext_vector_type(8)));

#define STAMP(K, i)
#define ST_AUX 2
#define LD_NT 0
#define PF_K1 1
#define PF_K2 0
#define DEPTH 4
#define POLL_SLEEP 8
#define REP_XCD 1
#define K1_PRE 1
#define K2_PRE 0

__device__ u32x2 row_load(__amdgpu_buffer_rsrc_t rsrc, int vindex, int voffset, int soffset, int aux) __asm("llvm.amdgcn.struct.ptr.buffer.load.v2i32");

__device__ __forceinline__ float b2f(u16 x) { return __uint_as_float(((u32)x) << 16); }
__device__ __forceinline__ float lo2f(u32 x) { return __uint_as_float(x << 16); }
__device__ __forceinline__ float hi2f(u32 x) { return __uint_as_float(x & 0xffff0000u); }
__device__ __forceinline__ float wel(const u32* __restrict__ w, int i) { return (i & 1) ? hi2f(w[i >> 1]) : lo2f(w[i >> 1]); }
__device__ __forceinline__ u32 pk2(float a, float b) {
    f32x2 v = {a, b};
    return __builtin_bit_cast(u32, __builtin_convertvector(v, bf16x2));
}
__device__ __forceinline__ float dot2(u32 a, u32 b, float c) {
    return __builtin_amdgcn_fdot2_f32_bf16(__builtin_bit_cast(bf16x2, a), __builtin_bit_cast(bf16x2, b), c, false);
}
__device__ __forceinline__ float rbf16(float x) { return lo2f(pk2(x, x)); }
__device__ __forceinline__ float fcos(float x) {
    return __builtin_amdgcn_cosf(__builtin_amdgcn_fractf(x * 0.15915494309189535f));
}
__device__ __forceinline__ void lds_barrier() { asm volatile("s_waitcnt lgkmcnt(0)\n\ts_barrier" ::: "memory"); }
__device__ __forceinline__ u32 bucket_of(u32 o) { return (u32)(((unsigned long long)(o & 0xffffffu) * 6871948u) >> 32); }
template <typename T> __device__ __forceinline__ T ldg(const void* p) {
    return *(const T*)p;
}

#define NE 160000
#define NT 2560000
#define NB 256
#define EPB 625
#define TPB 10000
#define NLUT 1024
#define BROWS 771
#define NBIN 208
#define WS_PRI 0
#define WS_SEC (NB * NB * 256)
#define WS_LCNT (WS_SEC + NB * NB * 128)
#define WS_FLAG (WS_LCNT + 1024)
#define WS_EPOCH (WS_FLAG + 32 * NB * 8)
#define WS_LIST (WS_EPOCH + 256)
#define LIST_BYTES (TPB * 16)
#define WS_END (WS_LIST + NB * LIST_BYTES)
#define SC1 16
typedef unsigned long long u64;
typedef __attribute__((address_space(1))) u64 gu64;

#define XROW 5000
#define XCAP 24
#define L_LUT 0
#define L_CNT (NLUT * 8)
#define L_BC (L_CNT + 2512)
#define L_G01 (L_BC + 1040)
#define L_G2 (L_G01 + 10000)
#define L_X (L_G2 + 10000)
#define L_OVF (L_X + XROW * XCAP)
#define L_END (L_OVF + 10000)
#define BSTR 260
#define L_SPILL (L_X + NBIN * BSTR)
#define NSPILL 512
#define L_TRASH (L_SPILL + NSPILL * 8)
#define L_ZERO (L_SPILL + 8192)

__device__ __forceinline__ u32 lut_base(u32 f01, u32 f2x) {
    u32 fm = max(max(f01 & 0x7fffu, (f01 >> 16) & 0x7fffu), f2x & 0x7fffu);
    int ea = 249 - (int)(fm >> 7);
    return (u32)min(max(ea, 0), 240) << 7;
}

#define KS 72
#define K_CNT L_X
#define K_FLAG (K_CNT + NB * 4 + 16)
#define K_BIN (K_FLAG + NB * 4)
#define K_W (K_BIN + NB * KS)
#define K_END (K_W + NB * KS * 4)
#define CSH 18
#define CSTEP ((1u << CSH) | 4u)
#define CFULL (64u << CSH)

__device__ __forceinline__ u32 mad_i24(u32 a, int b, u32 c) {
    u32 d;
    asm("v_mad_i32_i24 %0, %1, %2, %3" : "=v"(d) : "v"(a), "s"(b), "v"(c));
    return d;
}
template <int H> __device__ __forceinline__ u32 mad_half(u32 a, u32 b, u32 c) {
    u32 d;
    if (H) asm("v_mad_u32_u16 %0, %1, %2, %3 op_sel:[1,0,0,0]" : "=v"(d) : "v"(a), "s"(b), "v"(c));
    else asm("v_mad_u32_u16 %0, %1, %2, %3" : "=v"(d) : "v"(a), "s"(b), "v"(c));
    return d;
}
__device__ __forceinline__ u32 bin_of(u32 in) { return (u32)(((unsigned long long)(in & 0xffffffu) * (85u << 16)) >> 32); }
__device__ __forceinline__ void cell_put(char* lds, u32 ret, u32 w, u32 bin) {
    *(u32*)(lds + K_W + (ret & ((1u << CSH) - 1))) = w;
    *(u8*)(lds + K_BIN + ((ret >> 2) & ((1u << (CSH - 2)) - 1))) = (u8)bin;
}
struct List { __amdgpu_buffer_rsrc_t rs; u32 off; };
__device__ __forceinline__ void list_put(char* lds, List list, u32 in, u32 o, u32 a, u32 bk) {
    u32 p = atomicAdd((u32*)(lds + K_CNT + NB * 4), 1u);
    __builtin_amdgcn_raw_buffer_store_b128((u32x4){in, o, a, 0}, list.rs, p * 16, list.off, SC1);
    *(u32*)(lds + K_FLAG + bk * 4) = 128;
}
template <int N> __device__ __forceinline__ void part_chunk(char* lds, List list, const u32* o, const u32* in, const u32* an, u16x2 A02) {
    u32 tt[N / 2], bk[N], ret[N], w[N], bin[N];
    u32 any = 0;
#pragma unroll
    for (int p = 0; p < N / 2; ++p) {
        tt[p] = __builtin_bit_cast(u32, __builtin_elementwise_sub_sat(__builtin_bit_cast(u16x2, an[p] & 0x7fff7fffu), A02));
        any |= tt[p];
    }
#pragma unroll
    for (int k = 0; k < N; ++k) bk[k] = bucket_of(o[k]);
    if (__builtin_expect(__builtin_amdgcn_ballot_w64((any & 0xfc00fc00u) != 0) != 0, 0)) {
#pragma unroll
        for (int k = 0; k < N; ++k) {
            const u32 t = (k & 1) ? (tt[k >> 1] >> 16) : (tt[k >> 1] & 0xffffu), bn = bin_of(in[k]);
            u32 rt = CFULL;
            if (t < NLUT) rt = atomicAdd((u32*)(lds + K_CNT + bk[k] * 4), CSTEP);
            if (rt < CFULL) cell_put(lds, rt, ((o[k] - bk[k] * EPB) << 22) + (t << 10) + in[k] - bn * BROWS, bn);
            else list_put(lds, list, in[k], o[k], (k & 1) ? (an[k >> 1] & 0xffff0000u) : (an[k >> 1] << 16), bk[k]);
        }
        return;
    }
#pragma unroll
    for (int k = 0; k < N; ++k) ret[k] = atomicAdd((u32*)(lds + K_CNT + bk[k] * 4), CSTEP);
#pragma unroll
    for (int k = 0; k < N; ++k) {
        bin[k] = bin_of(in[k]);
        u32 x = (mad_i24(bk[k], -EPB, o[k]) << 22) + mad_i24(bin[k], -BROWS, in[k]);
        w[k] = (k & 1) ? mad_half<1>(tt[k >> 1], 1024u, x) : mad_half<0>(tt[k >> 1], 1024u, x);
    }
    u32 mx = ret[0];
#pragma unroll
    for (int k = 1; k < N; ++k) mx = max(mx, ret[k]);
    if (__builtin_expect(__builtin_amdgcn_ballot_w64(mx >= CFULL) == 0, 1)) {
#pragma unroll
        for (int k = 0; k < N; ++k) cell_put(lds, ret[k], w[k], bin[k]);
    } else {
#pragma unroll
        for (int k = 0; k < N; ++k) {
            if (ret[k] < CFULL) cell_put(lds, ret[k], w[k], bin[k]);
            else list_put(lds, list, in[k], o[k], (k & 1) ? (an[k >> 1] & 0xffff0000u) : (an[k >> 1] << 16), bk[k]);
        }
    }
}

__device__ __forceinline__ f32x4 down_wave(u32x2 wd, u32x2 mrow) {
    return __builtin_amdgcn_mfma_f32_4x4x4bf16_1k(__builtin_bit_cast(s16x4, wd), __builtin_bit_cast(s16x4, mrow), (f32x4){0.f, 0.f, 0.f, 0.f}, 0, 0, 0);
}
__device__ __forceinline__ f32x4 down_lane(const u32* __restrict__ w, u32x2 mrow) {
    float mm[4] = {lo2f(mrow.x), hi2f(mrow.x), lo2f(mrow.y), hi2f(mrow.y)};
    f32x4 md;
#pragma unroll
    for (int k = 0; k < 4; ++k) md[k] = fmaf(mm[3], wel(w, 12 + k), fmaf(mm[2], wel(w, 8 + k), fmaf(mm[1], wel(w, 4 + k), mm[0] * wel(w, k))));
    return md;
}
__device__ __forceinline__ f32x4 basis(u32x2 c, u32x4 g01, f32x4 g2) {
    float c2 = __uint_as_float(c.y);
    f32x2 P = f32x2{g2.x, g2.y} * c2, Q = f32x2{g2.z, g2.w} * c2;
    return (f32x4){dot2(c.x, g01.x, P.x), dot2(c.x, g01.y, P.y), dot2(c.x, g01.z, Q.x), dot2(c.x, g01.w, Q.y)};
}
__device__ __forceinline__ u32x2 mul_pack(f32x4 a, f32x4 b) {
    f32x2 P = f32x2{a.x, a.y} * f32x2{b.x, b.y}, Q = f32x2{a.z, a.w} * f32x2{b.z, b.w};
    return (u32x2){pk2(P.x, P.y), pk2(Q.x, Q.y)};
}
__device__ __forceinline__ void ovf_add(char* lds, u32 a16, u32x2 x) {
    float* p = (float*)(lds + L_OVF + a16);
    atomicAdd(p, lo2f(x.x)); atomicAdd(p + 1, hi2f(x.x)); atomicAdd(p + 2, lo2f(x.y)); atomicAdd(p + 3, hi2f(x.y));
}
__device__ __forceinline__ f32x4 rec_basis(char* lds, u32 r, u32& pos) {
    const u32 a4 = r >> 20, a16 = a4 << 2;
    pos = atomicAdd((u32*)(lds + L_CNT + a4), (u32)XROW);
    return basis(*(const u32x2*)(lds + L_LUT + ((r >> 7) & 0x1ff8u)), *(const u32x4*)(lds + L_G01 + a16), *(const f32x4*)(lds + L_G2 + a16));
}
__device__ __forceinline__ void rec_store(char* lds, u32 r, u32 pos, u32x2 x) {
    if (pos < L_OVF) *(u32x2*)(lds + pos) = x;
    else ovf_add(lds, (r >> 20) << 2, x);
}
template <int K> __device__ __forceinline__ u32 byte_x4(u32 v) {
    u32 d;
    if (K == 0) asm("v_lshlrev_b32_sdwa %0, 2, %1 dst_sel:DWORD dst_unused:UNUSED_PAD src0_sel:DWORD src1_sel:BYTE_0" : "=v"(d) : "v"(v));
    if (K == 1) asm("v_lshlrev_b32_sdwa %0, 2, %1 dst_sel:DWORD dst_unused:UNUSED_PAD src0_sel:DWORD src1_sel:BYTE_1" : "=v"(d) : "v"(v));
    if (K == 2) asm("v_lshlrev_b32_sdwa %0, 2, %1 dst_sel:DWORD dst_unused:UNUSED_PAD src0_sel:DWORD src1_sel:BYTE_2" : "=v"(d) : "v"(v));
    if (K == 3) asm("v_lshlrev_b32_sdwa %0, 2, %1 dst_sel:DWORD dst_unused:UNUSED_PAD src0_sel:DWORD src1_sel:BYTE_3" : "=v"(d) : "v"(v));
    return d;
}
__device__ __forceinline__ void bin_group(char* lds, u32 bb, u32x4 rec) {
    const u32 rr[4] = {rec.x, rec.y, rec.z, rec.w};
    const u32 ca[4] = {byte_x4<0>(bb), byte_x4<1>(bb), byte_x4<2>(bb), byte_x4<3>(bb)};
    u32 ret[4];
#pragma unroll
    for (int i = 0; i < 4; ++i) ret[i] = atomicAdd((u32*)(lds + L_BC + ca[i]), CSTEP);
    if (__builtin_expect(__builtin_amdgcn_ballot_w64(max(max(ret[0], ret[1]), max(ret[2], ret[3])) >= CFULL) == 0, 1)) {
#pragma unroll
        for (int i = 0; i < 4; ++i) *(u32*)(lds + (ret[i] & ((1u << CSH) - 1))) = rr[i];
    } else {
#pragma unroll
        for (int i = 0; i < 4; ++i) {
            if (ret[i] < CFULL) *(u32*)(lds + (ret[i] & ((1u << CSH) - 1))) = rr[i];
            else {
                u32 p = atomicAdd((u32*)(lds + L_BC + NB * 4), 1u);
                if (p < NSPILL) *(u32x2*)(lds + L_SPILL + p * 8) = (u32x2){rr[i], (ca[i] >> 2) * BROWS + (rr[i] & 1023u)};
            }
        }
    }
}

__global__ __launch_bounds__(1024, 4) void k_all(const u32x2* __restrict__ m, const int* __restrict__ idx_in,
                                              const int* __restrict__ idx_out, const u16* __restrict__ dist,
                                              const u16* __restrict__ angle, const u32* __restrict__ centers,
                                              const u32* __restrict__ freq, const u32* __restrict__ w_rbf,
                                              const u32* __restrict__ w_sbf, const u32* __restrict__ w_down,
                                              const u32* __restrict__ w_up, const u64* __restrict__ epoch,
                                              char* __restrict__ ws, u32x2* __restrict__ out) {
    __shared__ __attribute__((aligned(16))) char lds[L_END];
    const u32 tid = threadIdx.x, j = blockIdx.x;
    STAMP(0, 0);
    const u32 b = ((j & 7) + 8 * (j >> 7)) * 16 + ((j >> 3) & 15);
    const u32 lane = tid & 63, wv = __builtin_amdgcn_readfirstlane(tid >> 6);
    const u32 e = j * EPB + (tid < EPB ? tid : 0);
    const char* po = (const char*)(idx_out + b * TPB);
    const char* pi = (const char*)(idx_in + b * TPB);
    const char* pa = (const char*)(angle + b * TPB);
    const bool hasC = tid < 904;
    const u32 tc = hasC ? tid : 0;
    const u32x4 oA = ldg<u32x4>(po + tid * 16);
    const u32x2 aA = ldg<u32x2>(pa + tid * 8);
    const u32x4 iA = ldg<u32x4>(pi + tid * 16);
    __builtin_amdgcn_sched_barrier(0);
    u64 tag = *epoch;
    u32 f01 = freq[0], f2x = freq[1], cen[4], wu[8], w[8];
#pragma unroll
    for (int i = 0; i < 4; ++i) cen[i] = centers[i];
#pragma unroll
    for (int i = 0; i < 8; ++i) wu[i] = w_up[i], w[i] = w_down[i];
    __builtin_amdgcn_sched_barrier(0);
    {
        const u32 trash = (NBIN + b % (NB - NBIN)) * 0x01010101u;
        *(u32x4*)(lds + K_BIN + tid * 16) = (u32x4){trash, trash, trash, trash};
        if (tid < NB * KS / 16 - 1024) *(u32x4*)(lds + K_BIN + 16384 + tid * 16) = (u32x4){trash, trash, trash, trash};
        else if (tid - 256 <= NB) *(u32*)(lds + K_CNT + (tid - 256) * 4) = tid < 256 + NB ? (tid - 256) * (KS * 4) : 0;
        else if (tid - 576 < NB) *(u32*)(lds + K_FLAG + (tid - 576) * 4) = 0;
    }
    lds_barrier();
    asm volatile("" : "+s"(tag), "+s"(f01), "+s"(f2x), "+s"(cen[0]), "+s"(cen[1]), "+s"(cen[2]), "+s"(cen[3]));
    asm volatile("" : "+s"(wu[0]), "+s"(wu[1]), "+s"(wu[2]), "+s"(wu[3]), "+s"(wu[4]), "+s"(wu[5]), "+s"(wu[6]), "+s"(wu[7]));
    asm volatile("" : "+s"(w[0]), "+s"(w[1]), "+s"(w[2]), "+s"(w[3]), "+s"(w[4]), "+s"(w[5]), "+s"(w[6]), "+s"(w[7]));
    __builtin_amdgcn_sched_barrier(0);
    const u32x4 oB = ldg<u32x4>(po + 16384 + tid * 16);
    const u32x2 aB = ldg<u32x2>(pa + 8192 + tid * 8);
    const u32x4 iB = ldg<u32x4>(pi + 16384 + tid * 16);
    const u16 dist_e = dist[e];
    const u32 wsrc = *(lane < 48 ? w_sbf + lane : w_rbf + (lane - 48));
    __builtin_amdgcn_sched_barrier(0);
    const u32x2 oC = ldg<u32x2>(po + 32768 + tc * 8);
    const u32 aC = ldg<u32>(pa + 16384 + tc * 4);
    const u32x2 iC = ldg<u32x2>(pi + 32768 + tc * 8);
    __builtin_amdgcn_sched_barrier(0);
    STAMP(0, 1);
    tag = tag * 6364136223846793005ull + 1442695040888963407ull;
#pragma unroll
    for (int i = 0; i < 3; ++i) {
        u32 off = K_END + (i * 1024 + tid) * 16;
        if (i < 2 || off < L_END) *(u32x4*)(lds + off) = (u32x4){0, 0, 0, 0};
    }
    if (tid < EPB) *(u32*)(lds + L_CNT + tid * 4) = L_X + tid * 8;
    else if (tid - 704 <= NB) *(u32*)(lds + L_BC + (tid - 704) * 4) = tid < 704 + NBIN ? L_X + (tid - 704) * BSTR : tid < 704 + NB ? L_TRASH + (tid - 704 - NBIN) * 84 : 0;
    const float f0 = lo2f(f01), f1 = hi2f(f01), f2 = lo2f(f2x);
    const u32 A0 = lut_base(f01, f2x);
    {
        float a = lo2f(A0 + tid);
        u32 t01 = pk2(f0 * a, f1 * a);
        u32 c01 = pk2(fcos(lo2f(t01)), fcos(hi2f(t01)));
        float c2 = rbf16(fcos(rbf16(f2 * a)));
        *(u32x2*)(lds + L_LUT + tid * 8) = (u32x2){c01, __float_as_uint(c2)};
    }
    u32x2 wd;
    {
        const u32 sel = (lane & 1) ? 0x07060302u : 0x05040100u;
        const bool h = lane & 2;
        wd.x = __builtin_amdgcn_perm(h ? w[3] : w[2], h ? w[1] : w[0], sel);
        wd.y = __builtin_amdgcn_perm(h ? w[7] : w[6], h ? w[5] : w[4], sel);
        asm volatile("" : "+v"(wd));
    }
    asm volatile("" : "+s"(tag));
    __builtin_amdgcn_sched_barrier(0);
    const u16x2 A02 = {(u16)A0, (u16)A0};
    const __amdgpu_buffer_rsrc_t rs = __builtin_amdgcn_make_buffer_rsrc(ws, (short)0, WS_END, 0x00020000);
    const List list = {rs, WS_LIST + b * LIST_BYTES};
    {
        const u32 o[4] = {oA.x, oA.y, oA.z, oA.w}, in[4] = {iA.x, iA.y, iA.z, iA.w}, an[2] = {aA.x, aA.y};
        part_chunk<4>(lds, list, o, in, an, A02);
    }
    __builtin_amdgcn_sched_barrier(0);
    STAMP(0, 6);
    {
        const u32 o[4] = {oB.x, oB.y, oB.z, oB.w}, in[4] = {iB.x, iB.y, iB.z, iB.w}, an[2] = {aB.x, aB.y};
        part_chunk<4>(lds, list, o, in, an, A02);
    }
    __builtin_amdgcn_sched_barrier(0);
    STAMP(0, 7);
    u32 gate01 = 0, gate23 = 0;
    if (wv < 10) {
        char* wt = lds + L_G01 + wv * 1024;
        {
            u32 row = lane < 48 ? lane >> 1 : 0u, r = lane < 48 ? (row * 43u) >> 7 : (lane - 48u) >> 1, l = row - 3 * r, d = (lane & 1) * 2;
            u32 o0 = lane >= 48 ? d : (l == 2 ? 4 + d : 8 + 2 * d + l);
            u32 o1 = o0 + ((lane >= 48 || l == 2) ? 1 : 2);
            *(u16*)(wt + o0 * 16 + r * 2) = (u16)wsrc;
            *(u16*)(wt + o1 * 16 + r * 2) = (u16)(wsrc >> 16);
        }
        __builtin_amdgcn_fence(__ATOMIC_ACQ_REL, "wavefront");
        const u32 i = lane & 31;
        u32x4 A = *(const u32x4*)(wt + ((i & 3) + 4 * (i >> 3)) * 16);
        if (((i >> 2) & 1) != (lane >> 5)) A = (u32x4){0, 0, 0, 0};
        __builtin_amdgcn_fence(__ATOMIC_ACQ_REL, "wavefront");
        float d = b2f(dist_e);
        float rb[8];
#pragma unroll
        for (int r = 0; r < 8; ++r) {
            float df = d - wel(cen, r);
            rb[r] = __builtin_amdgcn_exp2f(df * df * -14.426950408889634f);
        }
        u32x4 B = {pk2(rb[0], rb[1]), pk2(rb[2], rb[3]), pk2(rb[4], rb[5]), pk2(rb[6], rb[7])};
        f32x16 D = {0, 0, 0, 0, 0, 0, 0, 0, 0, 0, 0, 0, 0, 0, 0, 0};
        D = __builtin_amdgcn_mfma_f32_32x32x16_bf16(__builtin_bit_cast(bf16x8, A), __builtin_bit_cast(bf16x8, B), D, 0, 0, 0);
        gate01 = pk2(D[0], D[1]); gate23 = pk2(D[2], D[3]);
        if (tid < EPB) {
            *(f32x4*)(lds + L_G2 + tid * 16) = (f32x4){D[4], D[5], D[6], D[7]};
            *(u32x4*)(lds + L_G01 + tid * 16) = (u32x4){pk2(D[8], D[9]), pk2(D[10], D[11]), pk2(D[12], D[13]), pk2(D[14], D[15])};
        }
    }
    __builtin_amdgcn_sched_barrier(0);
    STAMP(0, 8);
    if (hasC) {
        const u32 o[2] = {oC.x, oC.y}, in[2] = {iC.x, iC.y}, an[1] = {aC};
        part_chunk<2>(lds, list, o, in, an, A02);
    }
    __builtin_amdgcn_sched_barrier(0);
    asm volatile("" :: "v"(oC.x), "v"(aC), "v"(iC.x));
    const u32 warm = ((const u32*)m)[min((j >> 3) * 313 + min(tid, 312u), 9999u) * 32];
    __builtin_amdgcn_sched_barrier(0);
    STAMP(0, 2);
    lds_barrier();
    STAMP(0, 3);
    u32 big = 0;
    {
        const u32 sub = tid & 15, c0 = (tid >> 6) * 16 + ((tid >> 4) & 3);
        const u32 cs = sub < 12 ? KS * 4 : KS;
        const u32 src = (sub < 12 ? K_W + sub * 16 : K_BIN + (sub - 12) * 16) + c0 * cs;
        u32 hd[4];
        u32x4 v[4];
#pragma unroll
        for (int g = 0; g < 4; ++g) {
            const u32 c = c0 + g * 4;
            hd[g] = ((min(*(const u32*)(lds + K_CNT + c * 4) >> CSH, 64u) + 3) & ~3u) | *(const u32*)(lds + K_FLAG + c * 4);
            const u32x2 v0 = *(const u32x2*)(lds + src + g * 4 * cs), v1 = *(const u32x2*)(lds + src + g * 4 * cs + 8);
            v[g] = (u32x4){v0.x, v0.y, v1.x, v1.y};
        }
#pragma unroll
        for (int g = 0; g < 4; ++g) {
            if (sub == 15) v[g].x = hd[g];
            __builtin_amdgcn_raw_buffer_store_b128(v[g], rs, (c0 * NB + b) * 256 + sub * 16, g * 4 * NB * 256, SC1);
        }
#pragma unroll
        for (int g = 0; g < 4; ++g) {
            const u32 c = c0 + g * 4;
            if ((hd[g] & 127u) > 48 && sub < 5) {
                const u32 a = sub < 4 ? K_W + c * (KS * 4) + 192 + sub * 16 : K_BIN + c * KS + 48;
                const u32x2 v0 = *(const u32x2*)(lds + a), v1 = *(const u32x2*)(lds + a + 8);
                __builtin_amdgcn_raw_buffer_store_b128((u32x4){v0.x, v0.y, v1.x, v1.y}, rs, WS_SEC + (c * NB + b) * 128 + sub * 16, 0, SC1);
            }
        }
        if (wv == 15) {
            if (lane == 0) __builtin_amdgcn_raw_buffer_store_b32(*(const u32*)(lds + K_CNT + NB * 4), rs, WS_LCNT + b * 4, 0, SC1);
            const char* cp = lds + K_CNT + (((lane & 31) >> 2) + 64 * (lane & 3)) * 4;
#pragma unroll
            for (int k = 0; k < 8; ++k) big |= (*(const u32*)(cp + k * 32) >= (49u << CSH)) << k;
        }
    }
    asm volatile("" :: "v"(warm));
    STAMP(0, 4);
    asm volatile("s_waitcnt vmcnt(0) lgkmcnt(0)\n\ts_barrier" ::: "memory");
    if (wv == 15 && lane < 32)
        __hip_atomic_store((gu64*)(ws + WS_FLAG + lane * (NB * 8) + b * 8), (tag & ~255ull) | big, __ATOMIC_RELAXED, __HIP_MEMORY_SCOPE_AGENT);
    STAMP(0, 5);

    STAMP(1, 0);
    const u32 src = tid >> 2, sub = tid & 3;
#pragma unroll
    for (int i = 0; i < 2; ++i) {
        u32 off = L_ZERO + (i * 1024 + tid) * 16;
        if (i < 1 || off < K_END) *(u32x4*)(lds + off) = (u32x4){0, 0, 0, 0};
    }
    if (tid < NBIN) *(u32*)(lds + L_X + 256 + tid * BSTR) = 0;
    __builtin_amdgcn_sched_barrier(0);
    STAMP(1, 1);
    u64 fl;
    {
        const gu64* fp = (const gu64*)(ws + WS_FLAG + (REP_XCD ? (j & 7) * 4 + (j >> 6) : j >> 3) * (NB * 8) + src * 8);
        const u64 t0 = __builtin_amdgcn_s_memrealtime();
        for (;;) {
            fl = __hip_atomic_load(fp, __ATOMIC_RELAXED, __HIP_MEMORY_SCOPE_AGENT);
            if (__builtin_amdgcn_ballot_w64(((fl ^ tag) >> 8) != 0) == 0) break;
            if (__builtin_amdgcn_s_memrealtime() - t0 > 500000) return;
            __builtin_amdgcn_s_sleep(POLL_SLEEP);
        }
        asm volatile("" ::: "memory");
        __builtin_amdgcn_fence(__ATOMIC_ACQUIRE, "wavefront");
    }
    STAMP(1, 2);
    const u32 cell = WS_PRI + (j * NB + src) * 256, sec = WS_SEC + (j * NB + src) * 128;
    const u32x4 bn = __builtin_amdgcn_raw_buffer_load_b128(rs, cell + 192 + sub * 12, 0, SC1);
    __builtin_amdgcn_sched_barrier(0);
    u32x4 rec[4];
#pragma unroll
    for (int k = 0; k < 3; ++k) {
        rec[k] = __builtin_amdgcn_raw_buffer_load_b128(rs, cell + sub * 48 + k * 16, 0, SC1);
        __builtin_amdgcn_sched_barrier(0);
    }
    const u32 sec2 = ((u32)fl >> (REP_XCD ? (j >> 3) & 7 : j & 7)) & 1 ? sec : 0x7ff00000u;
    const u32 bn3 = __builtin_amdgcn_raw_buffer_load_b32(rs, sec2 + 64 + sub * 4, 0, SC1);
    rec[3] = __builtin_amdgcn_raw_buffer_load_b128(rs, sec2 + sub * 16, 0, SC1);
    asm volatile("" ::: "memory");
    __builtin_amdgcn_sched_barrier(0);
    const u32 hdr = __builtin_amdgcn_update_dpp(0u, bn.w, 0xff, 0xf, 0xf, true);
    const u32 n4 = hdr & 127u;
    asm volatile("" : "+v"(rec[0]));
    STAMP(1, 3);
    if (12 * sub < n4) bin_group(lds, bn.x, rec[0]);
    __builtin_amdgcn_sched_barrier(0);
    if (12 * sub + 4 < n4) bin_group(lds, bn.y, rec[1]);
    __builtin_amdgcn_sched_barrier(0);
    if (12 * sub + 8 < n4) bin_group(lds, bn.z, rec[2]);
    __builtin_amdgcn_sched_barrier(0);
    if (48 + 4 * sub < n4) bin_group(lds, bn3, rec[3]);
    __builtin_amdgcn_sched_barrier(0);
    STAMP(1, 4);
    lds_barrier();
    STAMP(1, 5);
    if (j == 0 && tid == 0) *(u64*)(ws + WS_EPOCH) = tag;
    const u32 cv = *(const u32*)(lds + L_BC + (lane < 13 ? wv * 13 + lane : NB) * 4);
    const __amdgpu_buffer_rsrc_t rm = __builtin_amdgcn_make_buffer_rsrc((void*)m, (short)8, NE * 8, 0x00020000);
    u32 r[13];
#pragma unroll
    for (int u = 0; u < 13; ++u) r[u] = atomicExch((u32*)(lds + L_X + wv * (13 * BSTR) + lane * 4 + u * BSTR), 0u);
    const u32x2 sp = *(const u32x2*)(lds + L_SPILL + tid * 8);
    *(u32x2*)(lds + L_SPILL + tid * 8) = (u32x2){0, 0};
    const u32 nspill = __builtin_amdgcn_readlane(cv, 13);
    u32x2 mrow = {0, 0};
    if (__builtin_expect(nspill <= NSPILL, 1)) {
        u32x2 mv[13];
        bool ok[13];
        const u32 lane1 = (lane + 1) << CSH;
#pragma unroll
        for (int u = 0; u < 13; ++u) ok[u] = lane1 <= __builtin_amdgcn_readlane(cv, u);
#pragma unroll
        for (int u = 0; u < DEPTH; ++u) mv[u] = row_load(rm, ok[u] ? r[u] & 1023u : 0xffffffu, 0, (wv * 13 + u) * (BROWS * 8), 0);
        __builtin_amdgcn_sched_barrier(0);
        lds_barrier();
        STAMP(1, 6);
#pragma unroll
        for (int u = 0; u < 13; ++u) {
            if (u + DEPTH < 13) mv[u + DEPTH] = row_load(rm, ok[u + DEPTH] ? r[u + DEPTH] & 1023u : 0xffffffu, 0, (wv * 13 + u + DEPTH) * (BROWS * 8), 0);
            if (u + DEPTH == 13) mrow = m[e];
            __builtin_amdgcn_sched_barrier(0);
            u32 pos;
            f32x4 s;
            asm volatile("" : "=v"(pos), "=v"(s));
            if (ok[u]) s = rec_basis(lds, r[u], pos);
            __builtin_amdgcn_sched_barrier(0);
            const f32x4 md = down_wave(wd, mv[u]);
            if (ok[u]) rec_store(lds, r[u], pos, mul_pack(s, md));
            __builtin_amdgcn_sched_barrier(0);
        }
        if (__builtin_expect(nspill > wv * 64, 0)) {
            if (tid < nspill) {
                u32 pos;
                const f32x4 s = rec_basis(lds, sp.x, pos);
                rec_store(lds, sp.x, pos, mul_pack(s, down_lane(w_down, m[sp.y])));
            }
        }
    } else {
        lds_barrier();
        mrow = m[e];
        for (u32 p = sub; p < n4; p += 4) {
            u32 rr = __builtin_amdgcn_raw_buffer_load_b32(rs, p < 48 ? cell + p * 4 : sec + (p - 48) * 4, 0, SC1);
            u32 bb = (__builtin_amdgcn_raw_buffer_load_b32(rs, (p < 48 ? cell + 192 : sec + 16) + (p & ~3u), 0, SC1) >> (8 * sub)) & 255u;
            if (bb < NBIN) {
                u32 pos;
                const f32x4 s = rec_basis(lds, rr, pos);
                rec_store(lds, rr, pos, mul_pack(s, down_lane(w_down, m[bb * BROWS + (rr & 1023u)])));
            }
        }
    }
    STAMP(1, 7);
    if (__builtin_expect((hdr & 128u) != 0, 0)) {
        const u32 n = __builtin_amdgcn_raw_buffer_load_b32(rs, WS_LCNT + src * 4, 0, SC1);
        for (u32 p = sub; p < n; p += 4) {
            u32x4 q = __builtin_amdgcn_raw_buffer_load_b128(rs, WS_LIST + src * LIST_BYTES + p * 16, 0, SC1);
            u32 lo = q.y - j * EPB;
            if (lo < EPB) {
                float a = __uint_as_float(q.z);
                u32x2 c = {pk2(cosf(rbf16(f0 * a)), cosf(rbf16(f1 * a))), __float_as_uint(rbf16(cosf(rbf16(f2 * a))))};
                ovf_add(lds, lo * 16, mul_pack(basis(c, *(const u32x4*)(lds + L_G01 + lo * 16), *(const f32x4*)(lds + L_G2 + lo * 16)), down_lane(w_down, m[q.x])));
            }
        }
    }
    lds_barrier();
    STAMP(1, 8);
    if (wv < 10) {
        f32x4 acc = *(const f32x4*)(lds + L_OVF + min(tid, EPB - 1u) * 16);
        const u32 i = lane & 15, g = lane >> 4;
        const u32 one = (i & 1) ? 0x3f800000u : 0x00003f80u;
        const u32 s01 = (i & 2) ? 0u : one, s23 = (i & 2) ? one : 0u;
        const bf16x8 A = __builtin_bit_cast(bf16x8, (u32x4){s01, s23, s01, s23});
        f32x4 sum[4];
#pragma unroll
        for (int q = 0; q < 4; ++q) {
            const char* xp = lds + L_X + g * (2 * XROW) + (wv * 64 + q * 16 + i) * 8;
            sum[q] = (f32x4){0.f, 0.f, 0.f, 0.f};
#pragma unroll
            for (int s = 0; s < 3; ++s) {
                u32x2 v0 = *(const u32x2*)(xp + s * 8 * XROW), v1 = *(const u32x2*)(xp + s * 8 * XROW + XROW);
                sum[q] = __builtin_amdgcn_mfma_f32_16x16x32_bf16(A, __builtin_bit_cast(bf16x8, (u32x4){v0.x, v0.y, v1.x, v1.y}), sum[q], 0, 0, 0);
            }
        }
        acc += g == 0 ? sum[0] : g == 1 ? sum[1] : g == 2 ? sum[2] : sum[3];
        u32 g01 = pk2(acc[0], acc[1]), g23 = pk2(acc[2], acc[3]);
        float ag[4] = {lo2f(g01), hi2f(g01), lo2f(g23), hi2f(g23)};
        float mm[4] = {lo2f(mrow.x), hi2f(mrow.x), lo2f(mrow.y), hi2f(mrow.y)};
        float gt[4] = {lo2f(gate01), hi2f(gate01), lo2f(gate23), hi2f(gate23)};
        float res[4];
#pragma unroll
        for (int k = 0; k < 4; ++k) {
            float u = 0.f;
#pragma unroll
            for (int i2 = 0; i2 < 4; ++i2) u = fmaf(ag[i2], wel(wu, i2 * 4 + k), u);
            res[k] = fmaf(u, gt[k], mm[k]);
        }
        if (tid < EPB) out[e] = (u32x2){pk2(res[0], res[1]), pk2(res[2], res[3])};
    }
    STAMP(1, 9);
}

extern "C" void kernel_launch(void* const* d_in, const int* in_sizes, int n_in,
                              void* d_out, int out_size, void* d_ws, size_t ws_size,
                              hipStream_t stream) {
    k_all<<<NB, 1024, 0, stream>>>((const u32x2*)d_in[0], (const int*)d_in[1], (const int*)d_in[2], (const u16*)d_in[3],
                                   (const u16*)d_in[4], (const u32*)d_in[5], (const u32*)d_in[6], (const u32*)d_in[7],
                                   (const u32*)d_in[8], (const u32*)d_in[9], (const u32*)d_in[10],
                                   (const u64*)((char*)d_ws + WS_EPOCH), (char*)d_ws, (u32x2*)d_out);
}
```

```cpp
#include <hip/hip_runtime.h>
#include <hip/hip_bf16.h>
#include <stdint.h>

typedef unsigned char u8;
typedef unsigned short u16;
typedef unsigned int u32;
typedef u16 u16x2 __attribute__((ext_vector_type(2)));
typedef short s16x4 __attribute__((ext_vector_type(4)));
typedef u32 u32x2 __attribute__((ext_vector_type(2)));
typedef u32 u32x3 __attribute__((ext_vector_type(3)));
typedef u32 u32x4 __attribute__((ext_vector_type(4)));
typedef float f32x2 __attribute__((ext_vector_type(2)));
typedef float f32x4 __attribute__((ext_vector_type(4)));
typedef float f32x16 __attribute__((ext_vector_type(16)));
typedef __bf16 bf16x2 __attribute__((ext_vector_type(2)));
typedef __bf16 bf16x8 __attribute__((ext_vector_type(8)));

#define DEPTH 3
#define PRE 2

__device__ u32x2 row_load(__amdgpu_buffer_rsrc_t rsrc, int vindex, int voffset, int soffset, int aux) __asm("llvm.amdgcn.struct.ptr.buffer.load.v2i32");

__device__ __forceinline__ float b2f(u16 x) { return __uint_as_float(((u32)x) << 16); }
__device__ __forceinline__ float lo2f(u32 x) { return __uint_as_float(x << 16); }
__device__ __forceinline__ float hi2f(u32 x) { return __uint_as_float(x & 0xffff0000u); }
__device__ __forceinline__ float wel(const u32* __restrict__ w, int i) { return (i & 1) ? hi2f(w[i >> 1]) : lo2f(w[i >> 1]); }
__device__ __forceinline__ u32 pk2(float a, float b) {
    f32x2 v = {a, b};
    return __builtin_bit_cast(u32, __builtin_convertvector(v, bf16x2));
}
__device__ __forceinline__ float dot2(u32 a, u32 b, float c) {
    return __builtin_amdgcn_fdot2_f32_bf16(__builtin_bit_cast(bf16x2, a), __builtin_bit_cast(bf16x2, b), c, false);
}
__device__ __forceinline__ float rbf16(float x) { return lo2f(pk2(x, x)); }
__device__ __forceinline__ float fcos(float x) {
    return __builtin_amdgcn_cosf(__builtin_amdgcn_fractf(x * 0.15915494309189535f));
}
__device__ __forceinline__ void lds_barrier() { asm volatile("s_waitcnt lgkmcnt(0)\n\ts_barrier" ::: "memory"); }
__device__ __forceinline__ u32 bucket_of(u32 o) { return (u32)(((unsigned long long)(o & 0xffffffu) * 6871948u) >> 32); }
template <typename T> __device__ __forceinline__ T ldg(const void* p) {
    return *(const T*)p;
}

#define NE 160000
#define NT 2560000
#define NB 256
#define EPB 625
#define TPB 10000
#define NLUT 1024
#define BROWS 771
#define NBIN 208
#define WS_PRI 0
#define WS_SEC (NB * NB * 256)
#define WS_LCNT (WS_SEC + NB * NB * 128)
#define WS_FLAG (WS_LCNT + 1024)
#define WS_EPOCH (WS_FLAG + 32 * NB * 8)
#define WS_LIST (WS_EPOCH + 256)
#define LIST_BYTES (TPB * 16)
#define WS_END (WS_LIST + NB * LIST_BYTES)
#define SC1 16
typedef unsigned long long u64;
typedef __attribute__((address_space(1))) u64 gu64;

#define XROW 5000
#define XCAP 24
#define L_LUT 0
#define L_CNT (NLUT * 8)
#define L_BC (L_CNT + 2512)
#define L_G01 (L_BC + 1040)
#define L_G2 (L_G01 + 10000)
#define L_X (L_G2 + 10000)
#define L_OVF (L_X + XROW * XCAP)
#define L_END (L_OVF + 10000)
#define BCAP 96
#define BSTR (BCAP * 4 + 4)
#define L_TRASH (L_X + NBIN * BSTR)
#define L_SPILL L_END
#define NSPILL 256
#define L_ALL (L_SPILL + NSPILL * 8)

__device__ __forceinline__ u32 lut_base(u32 f01, u32 f2x) {
    u32 fm = max(max(f01 & 0x7fffu, (f01 >> 16) & 0x7fffu), f2x & 0x7fffu);
    int ea = 249 - (int)(fm >> 7);
    return (u32)min(max(ea, 0), 240) << 7;
}

#define KS 72
#define K_CNT L_X
#define K_FLAG (K_CNT + NB * 4 + 16)
#define K_BIN (K_FLAG + NB * 4)
#define K_W (K_BIN + NB * KS)
#define K_END (K_W + NB * KS * 4)
#define CSH 18
#define CSTEP ((1u << CSH) | 4u)
#define CFULL ((u32)KS << CSH)

__device__ __forceinline__ u32 mad_i24(u32 a, int b, u32 c) {
    u32 d;
    asm("v_mad_i32_i24 %0, %1, %2, %3" : "=v"(d) : "v"(a), "s"(b), "v"(c));
    return d;
}
template <int H> __device__ __forceinline__ u32 mad_half(u32 a, u32 b, u32 c) {
    u32 d;
    if (H) asm("v_mad_u32_u16 %0, %1, %2, %3 op_sel:[1,0,0,0]" : "=v"(d) : "v"(a), "s"(b), "v"(c));
    else asm("v_mad_u32_u16 %0, %1, %2, %3" : "=v"(d) : "v"(a), "s"(b), "v"(c));
    return d;
}
__device__ __forceinline__ u32 bin_of(u32 in) { return (u32)(((unsigned long long)(in & 0xffffffu) * (85u << 16)) >> 32); }
__device__ __forceinline__ void cell_put(char* lds, u32 ret, u32 w, u32 bin) {
    *(u32*)(lds + K_W + (ret & ((1u << CSH) - 1))) = w;
    *(u8*)(lds + K_BIN + ((ret >> 2) & ((1u << (CSH - 2)) - 1))) = (u8)bin;
}
struct List { __amdgpu_buffer_rsrc_t rs; u32 off; };
__device__ __forceinline__ void list_put(char* lds, List list, u32 in, u32 o, u32 a, u32 bk) {
    u32 p = atomicAdd((u32*)(lds + K_CNT + NB * 4), 1u);
    __builtin_amdgcn_raw_buffer_store_b128((u32x4){in, o, a, 0}, list.rs, p * 16, list.off, SC1);
    *(u32*)(lds + K_FLAG + bk * 4) = 128;
}
template <int N> __device__ __forceinline__ void part_chunk(char* lds, List list, const u32* o, const u32* in, const u32* an, u16x2 A02) {
    u32 tt[N / 2], bk[N], ret[N], w[N], bin[N];
    u32 any = 0;
#pragma unroll
    for (int p = 0; p < N / 2; ++p) {
        tt[p] = __builtin_bit_cast(u32, __builtin_elementwise_sub_sat(__builtin_bit_cast(u16x2, an[p] & 0x7fff7fffu), A02));
        any |= tt[p];
    }
#pragma unroll
    for (int k = 0; k < N; ++k) bk[k] = bucket_of(o[k]);
    if (__builtin_expect(__builtin_amdgcn_ballot_w64((any & 0xfc00fc00u) != 0) != 0, 0)) {
#pragma unroll
        for (int k = 0; k < N; ++k) {
            const u32 t = (k & 1) ? (tt[k >> 1] >> 16) : (tt[k >> 1] & 0xffffu), bn = bin_of(in[k]);
            u32 rt = CFULL;
            if (t < NLUT) rt = atomicAdd((u32*)(lds + K_CNT + bk[k] * 4), CSTEP);
            if (rt < CFULL) cell_put(lds, rt, ((o[k] - bk[k] * EPB) << 22) + (t << 10) + in[k] - bn * BROWS, bn);
            else list_put(lds, list, in[k], o[k], (k & 1) ? (an[k >> 1] & 0xffff0000u) : (an[k >> 1] << 16), bk[k]);
        }
        return;
    }
#pragma unroll
    for (int k = 0; k < N; ++k) ret[k] = atomicAdd((u32*)(lds + K_CNT + bk[k] * 4), CSTEP);
#pragma unroll
    for (int k = 0; k < N; ++k) {
        bin[k] = bin_of(in[k]);
        u32 x = (mad_i24(bk[k], -EPB, o[k]) << 22) + mad_i24(bin[k], -BROWS, in[k]);
        w[k] = (k & 1) ? mad_half<1>(tt[k >> 1], 1024u, x) : mad_half<0>(tt[k >> 1], 1024u, x);
    }
    u32 mx = ret[0];
#pragma unroll
    for (int k = 1; k < N; ++k) mx = max(mx, ret[k]);
    if (__builtin_expect(__builtin_amdgcn_ballot_w64(mx >= CFULL) == 0, 1)) {
#pragma unroll
        for (int k = 0; k < N; ++k) cell_put(lds, ret[k], w[k], bin[k]);
    } else {
#pragma unroll
        for (int k = 0; k < N; ++k) {
            if (ret[k] < CFULL) cell_put(lds, ret[k], w[k], bin[k]);
            else list_put(lds, list, in[k], o[k], (k & 1) ? (an[k >> 1] & 0xffff0000u) : (an[k >> 1] << 16), bk[k]);
        }
    }
}

__device__ __forceinline__ f32x4 down_wave(u32x2 wd, u32x2 mrow) {
    return __builtin_amdgcn_mfma_f32_4x4x4bf16_1k(__builtin_bit_cast(s16x4, wd), __builtin_bit_cast(s16x4, mrow), (f32x4){0.f, 0.f, 0.f, 0.f}, 0, 0, 0);
}
__device__ __forceinline__ f32x4 down_lane(const u32* __restrict__ w, u32x2 mrow) {
    float mm[4] = {lo2f(mrow.x), hi2f(mrow.x), lo2f(mrow.y), hi2f(mrow.y)};
    f32x4 md;
#pragma unroll
    for (int k = 0; k < 4; ++k) md[k] = fmaf(mm[3], wel(w, 12 + k), fmaf(mm[2], wel(w, 8 + k), fmaf(mm[1], wel(w, 4 + k), mm[0] * wel(w, k))));
    return md;
}
__device__ __forceinline__ f32x4 basis(u32x2 c, u32x4 g01, f32x4 g2) {
    float c2 = __uint_as_float(c.y);
    f32x2 P = f32x2{g2.x, g2.y} * c2, Q = f32x2{g2.z, g2.w} * c2;
    return (f32x4){dot2(c.x, g01.x, P.x), dot2(c.x, g01.y, P.y), dot2(c.x, g01.z, Q.x), dot2(c.x, g01.w, Q.y)};
}
__device__ __forceinline__ u32x2 mul_pack(f32x4 a, f32x4 b) {
    f32x2 P = f32x2{a.x, a.y} * f32x2{b.x, b.y}, Q = f32x2{a.z, a.w} * f32x2{b.z, b.w};
    return (u32x2){pk2(P.x, P.y), pk2(Q.x, Q.y)};
}
__device__ __forceinline__ void ovf_add(char* lds, u32 a16, u32x2 x) {
    float* p = (float*)(lds + L_OVF + a16);
    atomicAdd(p, lo2f(x.x)); atomicAdd(p + 1, hi2f(x.x)); atomicAdd(p + 2, lo2f(x.y)); atomicAdd(p + 3, hi2f(x.y));
}
__device__ __forceinline__ f32x4 rec_basis(char* lds, u32 r, u32& pos) {
    const u32 a4 = r >> 20, a16 = a4 << 2;
    pos = atomicAdd((u32*)(lds + L_CNT + a4), (u32)XROW);
    return basis(*(const u32x2*)(lds + L_LUT + ((r >> 7) & 0x1ff8u)), *(const u32x4*)(lds + L_G01 + a16), *(const f32x4*)(lds + L_G2 + a16));
}
__device__ __forceinline__ void rec_store(char* lds, u32 r, u32 pos, u32x2 x) {
    if (pos < L_OVF) *(u32x2*)(lds + pos) = x;
    else ovf_add(lds, (r >> 20) << 2, x);
}
template <int K> __device__ __forceinline__ u32 byte_x4(u32 v) {
    u32 d;
    if (K == 0) asm("v_lshlrev_b32_sdwa %0, 2, %1 dst_sel:DWORD dst_unused:UNUSED_PAD src0_sel:DWORD src1_sel:BYTE_0" : "=v"(d) : "v"(v));
    if (K == 1) asm("v_lshlrev_b32_sdwa %0, 2, %1 dst_sel:DWORD dst_unused:UNUSED_PAD src0_sel:DWORD src1_sel:BYTE_1" : "=v"(d) : "v"(v));
    if (K == 2) asm("v_lshlrev_b32_sdwa %0, 2, %1 dst_sel:DWORD dst_unused:UNUSED_PAD src0_sel:DWORD src1_sel:BYTE_2" : "=v"(d) : "v"(v));
    if (K == 3) asm("v_lshlrev_b32_sdwa %0, 2, %1 dst_sel:DWORD dst_unused:UNUSED_PAD src0_sel:DWORD src1_sel:BYTE_3" : "=v"(d) : "v"(v));
    return d;
}
__device__ __forceinline__ void bin_piece(char* lds, u32x4 pc) {
    const u32 rr[3] = {pc.x, pc.y, pc.z};
    const u32 ca[3] = {byte_x4<0>(pc.w), byte_x4<1>(pc.w), byte_x4<2>(pc.w)};
    u32 ret[3];
#pragma unroll
    for (int i = 0; i < 3; ++i) ret[i] = atomicAdd((u32*)(lds + L_BC + ca[i]), CSTEP);
    if (__builtin_expect(__builtin_amdgcn_ballot_w64(max(max(ret[0], ret[1]), ret[2]) >= (BCAP << CSH)) == 0, 1)) {
#pragma unroll
        for (int i = 0; i < 3; ++i) *(u32*)(lds + (ret[i] & ((1u << CSH) - 1))) = rr[i];
    } else {
#pragma unroll
        for (int i = 0; i < 3; ++i) {
            if (ret[i] < (BCAP << CSH)) *(u32*)(lds + (ret[i] & ((1u << CSH) - 1))) = rr[i];
            else *(u32*)(lds + L_BC + NB * 4) = 1;
        }
    }
}
__device__ __forceinline__ u32x4 piece_of(const char* lds, u32 c, u32 p, u32 hd) {
    const u32* wp = (const u32*)(lds + K_W + c * (KS * 4) + p * 12);
    const u32* bp = (const u32*)(lds + K_BIN + c * KS + (p * 3 & ~3u));
    return (u32x4){wp[0], wp[1], wp[2], __builtin_amdgcn_perm(hd, __builtin_amdgcn_alignbyte(bp[1], bp[0], p * 3), 0x04020100u)};
}

__global__ __launch_bounds__(1024, 4) void k_all(const u32x2* __restrict__ m, const int* __restrict__ idx_in,
                                              const int* __restrict__ idx_out, const u16* __restrict__ dist,
                                              const u16* __restrict__ angle, const u32* __restrict__ centers,
                                              const u32* __restrict__ freq, const u32* __restrict__ w_rbf,
                                              const u32* __restrict__ w_sbf, const u32* __restrict__ w_down,
                                              const u32* __restrict__ w_up, const u64* __restrict__ epoch,
                                              char* __restrict__ ws, u32x2* __restrict__ out) {
    __shared__ __attribute__((aligned(16))) char lds[L_ALL];
    const u32 tid = threadIdx.x, j = blockIdx.x;
    const u32 b = ((j & 7) + 8 * (j >> 7)) * 16 + ((j >> 3) & 15);
    const u32 lane = tid & 63, wv = __builtin_amdgcn_readfirstlane(tid >> 6);
    const u32 e = j * EPB + (tid < EPB ? tid : 0);
    const char* po = (const char*)(idx_out + b * TPB);
    const char* pi = (const char*)(idx_in + b * TPB);
    const char* pa = (const char*)(angle + b * TPB);
    const bool hasC = tid < 904;
    const u32 tc = hasC ? tid : 0;
    const u32x4 oA = ldg<u32x4>(po + tid * 16);
    const u32x2 aA = ldg<u32x2>(pa + tid * 8);
    const u32x4 iA = ldg<u32x4>(pi + tid * 16);
    __builtin_amdgcn_sched_barrier(0);
    u64 tag = *epoch;
    u32 f01 = freq[0], f2x = freq[1], cen[4], wu[8], w[8];
#pragma unroll
    for (int i = 0; i < 4; ++i) cen[i] = centers[i];
#pragma unroll
    for (int i = 0; i < 8; ++i) wu[i] = w_up[i], w[i] = w_down[i];
    __builtin_amdgcn_sched_barrier(0);
    {
        const u32 trash = (NBIN + b % (NB - NBIN)) * 0x01010101u;
        *(u32x4*)(lds + K_BIN + tid * 16) = (u32x4){trash, trash, trash, trash};
        if (tid < NB * KS / 16 - 1024) *(u32x4*)(lds + K_BIN + 16384 + tid * 16) = (u32x4){trash, trash, trash, trash};
        else if (tid - 256 <= NB) *(u32*)(lds + K_CNT + (tid - 256) * 4) = tid < 256 + NB ? (tid - 256) * (KS * 4) : 0;
        else if (tid - 576 < NB) *(u32*)(lds + K_FLAG + (tid - 576) * 4) = 0;
    }
    lds_barrier();
    asm volatile("" : "+s"(tag), "+s"(f01), "+s"(f2x), "+s"(cen[0]), "+s"(cen[1]), "+s"(cen[2]), "+s"(cen[3]));
    asm volatile("" : "+s"(wu[0]), "+s"(wu[1]), "+s"(wu[2]), "+s"(wu[3]), "+s"(wu[4]), "+s"(wu[5]), "+s"(wu[6]), "+s"(wu[7]));
    asm volatile("" : "+s"(w[0]), "+s"(w[1]), "+s"(w[2]), "+s"(w[3]), "+s"(w[4]), "+s"(w[5]), "+s"(w[6]), "+s"(w[7]));
    __builtin_amdgcn_sched_barrier(0);
    const u32x4 oB = ldg<u32x4>(po + 16384 + tid * 16);
    const u32x2 aB = ldg<u32x2>(pa + 8192 + tid * 8);
    const u32x4 iB = ldg<u32x4>(pi + 16384 + tid * 16);
    const u16 dist_e = dist[e];
    const u32 wsrc = *(lane < 48 ? w_sbf + lane : w_rbf + (lane - 48));
    __builtin_amdgcn_sched_barrier(0);
    const u32x2 oC = ldg<u32x2>(po + 32768 + tc * 8);
    const u32 aC = ldg<u32>(pa + 16384 + tc * 4);
    const u32x2 iC = ldg<u32x2>(pi + 32768 + tc * 8);
    __builtin_amdgcn_sched_barrier(0);
    tag = tag * 6364136223846793005ull + 1442695040888963407ull;
#pragma unroll
    for (int i = 0; i < 3; ++i) {
        u32 off = K_END + (i * 1024 + tid) * 16;
        if (i < 2 || off < L_END) *(u32x4*)(lds + off) = (u32x4){0, 0, 0, 0};
    }
    if (tid >= 1024 - 128) *(u32x4*)(lds + L_SPILL + (tid - (1024 - 128)) * 16) = (u32x4){0, ~0u, 0, ~0u};
    if (tid < EPB) *(u32*)(lds + L_CNT + tid * 4) = L_X + tid * 8;
    else if (tid - 704 <= NB + 1) *(u32*)(lds + L_BC + (tid - 704) * 4) = tid < 704 + NBIN ? L_X + (tid - 704) * BSTR : tid < 704 + NB ? L_TRASH + (tid - 704 - NBIN) * 84 : 0;
    const float f0 = lo2f(f01), f1 = hi2f(f01), f2 = lo2f(f2x);
    const u32 A0 = lut_base(f01, f2x);
    {
        float a = lo2f(A0 + tid);
        u32 t01 = pk2(f0 * a, f1 * a);
        u32 c01 = pk2(fcos(lo2f(t01)), fcos(hi2f(t01)));
        float c2 = rbf16(fcos(rbf16(f2 * a)));
        *(u32x2*)(lds + L_LUT + tid * 8) = (u32x2){c01, __float_as_uint(c2)};
    }
    u32x2 wd;
    {
        const u32 sel = (lane & 1) ? 0x07060302u : 0x05040100u;
        const bool h = lane & 2;
        wd.x = __builtin_amdgcn_perm(h ? w[3] : w[2], h ? w[1] : w[0], sel);
        wd.y = __builtin_amdgcn_perm(h ? w[7] : w[6], h ? w[5] : w[4], sel);
        asm volatile("" : "+v"(wd));
    }
    asm volatile("" : "+s"(tag));
    __builtin_amdgcn_sched_barrier(0);
    const u16x2 A02 = {(u16)A0, (u16)A0};
    const __amdgpu_buffer_rsrc_t rs = __builtin_amdgcn_make_buffer_rsrc(ws, (short)0, WS_END, 0x00020000);
    const List list = {rs, WS_LIST + b * LIST_BYTES};
    {
        const u32 o[4] = {oA.x, oA.y, oA.z, oA.w}, in[4] = {iA.x, iA.y, iA.z, iA.w}, an[2] = {aA.x, aA.y};
        part_chunk<4>(lds, list, o, in, an, A02);
    }
    __builtin_amdgcn_sched_barrier(0);
    {
        const u32 o[4] = {oB.x, oB.y, oB.z, oB.w}, in[4] = {iB.x, iB.y, iB.z, iB.w}, an[2] = {aB.x, aB.y};
        part_chunk<4>(lds, list, o, in, an, A02);
    }
    __builtin_amdgcn_sched_barrier(0);
    u32 gate01 = 0, gate23 = 0;
    if (wv < 10) {
        char* wt = lds + L_G01 + wv * 1024;
        {
            u32 row = lane < 48 ? lane >> 1 : 0u, r = lane < 48 ? (row * 43u) >> 7 : (lane - 48u) >> 1, l = row - 3 * r, d = (lane & 1) * 2;
            u32 o0 = lane >= 48 ? d : (l == 2 ? 4 + d : 8 + 2 * d + l);
            u32 o1 = o0 + ((lane >= 48 || l == 2) ? 1 : 2);
            *(u16*)(wt + o0 * 16 + r * 2) = (u16)wsrc;
            *(u16*)(wt + o1 * 16 + r * 2) = (u16)(wsrc >> 16);
        }
        __builtin_amdgcn_fence(__ATOMIC_ACQ_REL, "wavefront");
        const u32 i = lane & 31;
        u32x4 A = *(const u32x4*)(wt + ((i & 3) + 4 * (i >> 3)) * 16);
        if (((i >> 2) & 1) != (lane >> 5)) A = (u32x4){0, 0, 0, 0};
        __builtin_amdgcn_fence(__ATOMIC_ACQ_REL, "wavefront");
        float d = b2f(dist_e);
        float rb[8];
#pragma unroll
        for (int r = 0; r < 8; ++r) {
            float df = d - wel(cen, r);
            rb[r] = __builtin_amdgcn_exp2f(df * df * -14.426950408889634f);
        }
        u32x4 B = {pk2(rb[0], rb[1]), pk2(rb[2], rb[3]), pk2(rb[4], rb[5]), pk2(rb[6], rb[7])};
        f32x16 D = {0, 0, 0, 0, 0, 0, 0, 0, 0, 0, 0, 0, 0, 0, 0, 0};
        D = __builtin_amdgcn_mfma_f32_32x32x16_bf16(__builtin_bit_cast(bf16x8, A), __builtin_bit_cast(bf16x8, B), D, 0, 0, 0);
        gate01 = pk2(D[0], D[1]); gate23 = pk2(D[2], D[3]);
        if (tid < EPB) {
            *(f32x4*)(lds + L_G2 + tid * 16) = (f32x4){D[4], D[5], D[6], D[7]};
            *(u32x4*)(lds + L_G01 + tid * 16) = (u32x4){pk2(D[8], D[9]), pk2(D[10], D[11]), pk2(D[12], D[13]), pk2(D[14], D[15])};
        }
    }
    __builtin_amdgcn_sched_barrier(0);
    if (hasC) {
        const u32 o[2] = {oC.x, oC.y}, in[2] = {iC.x, iC.y}, an[1] = {aC};
        part_chunk<2>(lds, list, o, in, an, A02);
    }
    __builtin_amdgcn_sched_barrier(0);
    asm volatile("" :: "v"(oC.x), "v"(aC), "v"(iC.x));
    const u32 warm = ((const u32*)m)[min((j >> 3) * 313 + min(tid, 312u), 9999u) * 32];
    __builtin_amdgcn_sched_barrier(0);
    lds_barrier();
    u32 big = 0;
    {
        const u32 sub = tid & 15, c0 = (tid >> 6) * 16 + ((tid >> 4) & 3);
        u32 hd[4];
        u32x4 v[4];
#pragma unroll
        for (int g = 0; g < 4; ++g) {
            const u32 c = c0 + g * 4;
            hd[g] = min(*(const u32*)(lds + K_CNT + c * 4) >> CSH, (u32)KS) | *(const u32*)(lds + K_FLAG + c * 4);
            v[g] = piece_of(lds, c, sub, hd[g]);
        }
#pragma unroll
        for (int g = 0; g < 4; ++g) __builtin_amdgcn_raw_buffer_store_b128(v[g], rs, (sub & ~3u) * 3 < max(hd[g] & 127u, 1u) ? (c0 * NB + b) * 256 + sub * 16 : 0x7ff00000u, g * 4 * NB * 256, SC1);
#pragma unroll
        for (int g = 0; g < 4; ++g) {
            const u32 c = c0 + g * 4;
            if ((hd[g] & 127u) > 48 && sub < ((hd[g] & 127u) <= 60 ? 4 : 8)) __builtin_amdgcn_raw_buffer_store_b128(piece_of(lds, c, 16 + sub, hd[g]), rs, WS_SEC + (c * NB + b) * 128 + sub * 16, 0, SC1);
        }
        if (wv == 15) {
            if (lane == 0) __builtin_amdgcn_raw_buffer_store_b32(*(const u32*)(lds + K_CNT + NB * 4), rs, WS_LCNT + b * 4, 0, SC1);
            const char* cp = lds + K_CNT + (((lane & 31) >> 2) + 64 * (lane & 3)) * 4;
#pragma unroll
            for (int k = 0; k < 8; ++k) big |= (*(const u32*)(cp + k * 32) >= (49u << CSH)) << k;
        }
    }
    asm volatile("" :: "v"(warm));
    asm volatile("s_waitcnt vmcnt(0) lgkmcnt(0)\n\ts_barrier" ::: "memory");
    if (wv == 15 && lane < 32)
        __hip_atomic_store((gu64*)(ws + WS_FLAG + lane * (NB * 8) + b * 8), (tag & ~255ull) | big, __ATOMIC_RELAXED, __HIP_MEMORY_SCOPE_AGENT);

    const u32 src = tid >> 2, sub = tid & 3;
#pragma unroll
    for (int i = 0; i < 6; ++i) {
        u32 off = L_X + (i * 1024 + tid) * 16;
        if (i < 5 || off < K_END) *(u32x4*)(lds + off) = (u32x4){0, 0, 0, 0};
    }
    lds_barrier();
    __builtin_amdgcn_sched_barrier(0);
    u64 fl;
    {
        const gu64* fp = (const gu64*)(ws + WS_FLAG + ((j & 7) * 4 + (j >> 6)) * (NB * 8) + src * 8);
        const u64 t0 = __builtin_amdgcn_s_memrealtime();
        for (;;) {
            fl = __hip_atomic_load(fp, __ATOMIC_RELAXED, __HIP_MEMORY_SCOPE_AGENT);
            if (__builtin_amdgcn_ballot_w64(((fl ^ tag) >> 8) != 0) == 0) break;
            if (__builtin_amdgcn_s_memrealtime() - t0 > 500000) return;
            __builtin_amdgcn_s_sleep(8);
        }
        asm volatile("" ::: "memory");
        __builtin_amdgcn_fence(__ATOMIC_ACQUIRE, "wavefront");
    }
    const u32 cell = WS_PRI + (j * NB + wv * 16) * 256 + lane * 16;
    const u32 sec = ((u32)fl >> ((j >> 3) & 7)) & 1 ? WS_SEC + (j * NB + src) * 128 + sub * 16 : 0x7ff00000u;
    const u32 sec2 = sec + 64;
    u32x4 pc[6];
#pragma unroll
    for (int k = 0; k < 4; ++k) {
        pc[k] = __builtin_amdgcn_raw_buffer_load_b128(rs, cell + k * 1024, 0, SC1);
        __builtin_amdgcn_sched_barrier(0);
    }
    pc[4] = __builtin_amdgcn_raw_buffer_load_b128(rs, sec, 0, SC1);
    pc[5] = __builtin_amdgcn_raw_buffer_load_b128(rs, sec2, 0, SC1);
    asm volatile("" ::: "memory");
    __builtin_amdgcn_sched_barrier(0);
    asm volatile("" : "+v"(pc[0]));
    const u32 first = (lane & 15) * 3;
#define HDR(x) ((u32)__builtin_amdgcn_update_dpp(0u, (x), 0x150, 0xf, 0xf, true))
    u32 hw[6];
#pragma unroll
    for (int k = 0; k < 6; ++k) {
        hw[k] = k < 4 ? HDR(pc[k].w) : pc[4].w;
        if ((k < 4 ? first : 48 + 12 * (k - 4) + 3 * sub) < ((hw[k] >> 24) & 127u)) bin_piece(lds, pc[k]);
        __builtin_amdgcn_sched_barrier(0);
    }
    u32 lists = __builtin_amdgcn_perm(__builtin_amdgcn_perm(hw[3], hw[2], 0x07030703u), __builtin_amdgcn_perm(hw[1], hw[0], 0x07030703u), 0x05040100u) & 0x80808080u;
    asm volatile("" : "+v"(lists));
    lds_barrier();
    const u32 cv = *(const u32*)(lds + L_BC + (wv * 13 + min(lane, 12u)) * 4);
    const __amdgpu_buffer_rsrc_t rm = __builtin_amdgcn_make_buffer_rsrc((void*)m, (short)8, NE * 8, 0x00020000);
    u32 r[13];
#pragma unroll
    for (int u = 0; u < 13; ++u) r[u] = atomicExch((u32*)(lds + L_X + wv * (13 * BSTR) + lane * 4 + u * BSTR), 0u);
    *(u32*)(lds + L_TRASH + tid * 4) = 0;
    u32x2 mrow = {0, 0};
    u32x2 mv[13];
    bool ok[13];
    const u32 lane1 = (lane + 1) << CSH;
#pragma unroll
    for (int u = 0; u < 13; ++u) ok[u] = lane1 <= __builtin_amdgcn_readlane(cv, u);
#pragma unroll
    for (int u = 0; u < PRE; ++u) mv[u] = row_load(rm, ok[u] ? r[u] & 1023u : 0xffffffu, 0, (wv * 13 + u) * (BROWS * 8), 0);
    __builtin_amdgcn_sched_barrier(0);
    u32 over = (u32)__builtin_amdgcn_ballot_w64(cv >= (65u << CSH)) & 0x1fffu;
    while (over != 0) {
        const u32 u = __builtin_ctz(over), bin = wv * 13 + u, more = min((u32)__builtin_amdgcn_readlane(cv, u) >> CSH, (u32)BCAP) - 64;
        over &= over - 1;
        u32 at = 0;
        if (lane == 0) at = atomicAdd((u32*)(lds + L_BC + NB * 4 + 4), more);
        at = __builtin_amdgcn_readfirstlane(at) + lane;
        if (lane < more) {
            const u32 rr = atomicExch((u32*)(lds + L_X + bin * BSTR + 256 + lane * 4), 0u);
            if (at < NSPILL) *(u32x2*)(lds + L_SPILL + at * 8) = (u32x2){rr, bin * BROWS + (rr & 1023u)};
            else *(u32*)(lds + L_BC + NB * 4) = 1;
        }
    }
    lds_barrier();
    if (__builtin_expect(__builtin_amdgcn_readfirstlane(*(const u32*)(lds + L_BC + NB * 4)) == 0, 1)) {
#pragma unroll
        for (int u = PRE; u < DEPTH; ++u) mv[u] = row_load(rm, ok[u] ? r[u] & 1023u : 0xffffffu, 0, (wv * 13 + u) * (BROWS * 8), 0);
#pragma unroll
        for (int u = 0; u < 13; ++u) {
            if (u + DEPTH < 13) mv[u + DEPTH] = row_load(rm, ok[u + DEPTH] ? r[u + DEPTH] & 1023u : 0xffffffu, 0, (wv * 13 + u + DEPTH) * (BROWS * 8), 0);
            if (u + DEPTH == 13) mrow = m[e];
            __builtin_amdgcn_sched_barrier(0);
            u32 pos;
            f32x4 s;
            asm volatile("" : "=v"(pos), "=v"(s));
            if (ok[u]) s = rec_basis(lds, r[u], pos);
            __builtin_amdgcn_sched_barrier(0);
            const f32x4 md = down_wave(wd, mv[u]);
            if (ok[u]) rec_store(lds, r[u], pos, mul_pack(s, md));
            __builtin_amdgcn_sched_barrier(0);
        }
        if (wv < 4) {
            const u32x2 sp = *(const u32x2*)(lds + L_SPILL + tid * 8);
            if (sp.y != ~0u) {
                u32 pos;
                const f32x4 s = rec_basis(lds, sp.x, pos);
                rec_store(lds, sp.x, pos, mul_pack(s, down_lane(w_down, m[sp.y])));
            }
        }
    } else {
        for (u32 off = L_X + tid * 16; off < L_END; off += 16384) *(u32x4*)(lds + off) = (u32x4){0, 0, 0, 0};
        lds_barrier();
        mrow = m[e];
        u32 n = 0;
        for (u32 k = 0; k < 6; ++k) {
            const u32x4 q = __builtin_amdgcn_raw_buffer_load_b128(rs, k < 4 ? cell + k * 1024 : k == 4 ? sec : sec2, 0, SC1);
            const u32 p0 = k < 4 ? first : 48 + 12 * (k - 4) + 3 * sub;
            const u32 row = HDR(q.w);
            if (k < 5) n = ((k < 4 ? row : q.w) >> 24) & 127u;
            for (u32 i = 0; i < 3; ++i) {
                if (p0 + i < n) {
                    const u32 rr = i == 0 ? q.x : i == 1 ? q.y : q.z;
                    u32 pos;
                    const f32x4 s = rec_basis(lds, rr, pos);
                    rec_store(lds, rr, pos, mul_pack(s, down_lane(w_down, m[((q.w >> (8 * i)) & 255u) * BROWS + (rr & 1023u)])));
                }
            }
        }
    }
    if (__builtin_expect(lists != 0, 0)) {
        for (u32 k = 0; k < 4; ++k) {
            if (!((lists >> (8 * k)) & 128u)) continue;
            const u32 sb = wv * 16 + k * 4 + (lane >> 4);
            const u32 n = min(__builtin_amdgcn_raw_buffer_load_b32(rs, WS_LCNT + sb * 4, 0, SC1), (u32)TPB);
            for (u32 p = lane & 15; p < n; p += 16) {
                u32x4 q = __builtin_amdgcn_raw_buffer_load_b128(rs, WS_LIST + sb * LIST_BYTES + p * 16, 0, SC1);
                u32 lo = q.y - j * EPB;
                if (lo < EPB) {
                    float a = __uint_as_float(q.z);
                    u32x2 c = {pk2(cosf(rbf16(f0 * a)), cosf(rbf16(f1 * a))), __float_as_uint(rbf16(cosf(rbf16(f2 * a))))};
                    ovf_add(lds, lo * 16, mul_pack(basis(c, *(const u32x4*)(lds + L_G01 + lo * 16), *(const f32x4*)(lds + L_G2 + lo * 16)), down_lane(w_down, m[q.x])));
                }
            }
        }
    }
    lds_barrier();
    if (wv < 10) {
        f32x4 acc = *(const f32x4*)(lds + L_OVF + min(tid, EPB - 1u) * 16);
        const u32 i = lane & 15, g = lane >> 4;
        const u32 one = (i & 1) ? 0x3f800000u : 0x00003f80u;
        const u32 s01 = (i & 2) ? 0u : one, s23 = (i & 2) ? one : 0u;
        const bf16x8 A = __builtin_bit_cast(bf16x8, (u32x4){s01, s23, s01, s23});
        f32x4 sum[4];
#pragma unroll
        for (int q = 0; q < 4; ++q) {
            const char* xp = lds + L_X + g * (2 * XROW) + (wv * 64 + q * 16 + i) * 8;
            sum[q] = (f32x4){0.f, 0.f, 0.f, 0.f};
#pragma unroll
            for (int s = 0; s < 3; ++s) {
                u32x2 v0 = *(const u32x2*)(xp + s * 8 * XROW), v1 = *(const u32x2*)(xp + s * 8 * XROW + XROW);
                sum[q] = __builtin_amdgcn_mfma_f32_16x16x32_bf16(A, __builtin_bit_cast(bf16x8, (u32x4){v0.x, v0.y, v1.x, v1.y}), sum[q], 0, 0, 0);
            }
        }
        acc += g == 0 ? sum[0] : g == 1 ? sum[1] : g == 2 ? sum[2] : sum[3];
        u32 g01 = pk2(acc[0], acc[1]), g23 = pk2(acc[2], acc[3]);
        float ag[4] = {lo2f(g01), hi2f(g01), lo2f(g23), hi2f(g23)};
        float mm[4] = {lo2f(mrow.x), hi2f(mrow.x), lo2f(mrow.y), hi2f(mrow.y)};
        float gt[4] = {lo2f(gate01), hi2f(gate01), lo2f(gate23), hi2f(gate23)};
        float res[4];
#pragma unroll
        for (int k = 0; k < 4; ++k) {
            float u = 0.f;
#pragma unroll
            for (int i2 = 0; i2 < 4; ++i2) u = fmaf(ag[i2], wel(wu, i2 * 4 + k), u);
            res[k] = fmaf(u, gt[k], mm[k]);
        }
        if (tid < EPB) out[e] = (u32x2){pk2(res[0], res[1]), pk2(res[2], res[3])};
    }
    if (j == 0 && tid == 1023) *(u64*)(ws + WS_EPOCH) = tag;
}

extern "C" void kernel_launch(void* const* d_in, const int* in_sizes, int n_in,
                              void* d_out, int out_size, void* d_ws, size_t ws_size,
                              hipStream_t stream) {
    k_all<<<NB, 1024, 0, stream>>>((const u32x2*)d_in[0], (const int*)d_in[1], (const int*)d_in[2], (const u16*)d_in[3],
                                   (const u16*)d_in[4], (const u32*)d_in[5], (const u32*)d_in[6], (const u32*)d_in[7],
                                   (const u32*)d_in[8], (const u32*)d_in[9], (const u32*)d_in[10],
                                   (const u64*)((char*)d_ws + WS_EPOCH), (char*)d_ws, (u32x2*)d_out);
}
```
